```python
import torch

def _softmax(x, axis=-1):
    return torch.softmax(x, dim=axis)

def _gelu(x):
    return torch.nn.functional.gelu(x, approximate='tanh')

def _silu(x):
    return torch.nn.functional.silu(x)

def _sigmoid(x):
    return torch.sigmoid(x)

def _layernorm(x, axis=-1, eps=1e-05):
    m = x.mean(dim=axis, keepdim=True)
    v = x.var(dim=axis, keepdim=True, unbiased=False)
    return (x - m) / torch.sqrt(v + eps)

def _scatter_add(tgt, idx, src, axis=0):
    return tgt.clone().index_add_(axis, idx, src)

def _segment_sum(src, seg, n):
    out = torch.zeros((n,) + src.shape[1:], dtype=src.dtype)
    return out.index_add_(0, seg, src)

def _pad(x, pw):
    flat = tuple((v for pair in reversed(pw) for v in pair))
    return torch.nn.functional.pad(x, flat)
kernel_name = 'adaLN_cond__gqa_block_cached__dense_activation_silu__decode'

def setup_inputs():

    _w = lambda a: (lambda t: t.to(torch.bfloat16) if t.is_floating_point() else t)(torch.as_tensor(a))

    def _A_setup():
        B, Sq, D = (1, 1, 576)
        Hq, Hkv, Sk, Dh = (32, 16, 8192, 128)
        assert Hq % Hkv == 0, (Hq, Hkv)
        return {'x': _w((lambda *s: torch.randn(*s, dtype=torch.bfloat16) * 0.1)(B, Sq, D)), 'k_cache': _w((lambda *s: torch.randn(*s, dtype=torch.bfloat16) * 1.6)(B, Hkv, Sk, Dh)), 'v_cache': _w((lambda *s: torch.randn(*s, dtype=torch.bfloat16) * 3.2)(B, Hkv, Sk, Dh)), 'wq': _w((lambda *s: torch.randn(*s, dtype=torch.bfloat16) * 1.6)(D, Hq * Dh) * D ** (-0.5)), 'wo': _w((lambda *s: torch.randn(*s, dtype=torch.bfloat16) * 3.2)(Hq * Dh, D) * (Hq * Dh) ** (-0.5))}
    _A_in = _A_setup()

    def _B_setup():
        B, S, D, F = (1, 1, 576, 4608)
        return {'x': _w((lambda *s: torch.randn(*s, dtype=torch.bfloat16) * 0.1)(B, S, D)), 'w1': _w((lambda *s: torch.randn(*s, dtype=torch.bfloat16) * 3.2)(D, F) * D ** (-0.5)), 'w2': _w((lambda *s: torch.randn(*s, dtype=torch.bfloat16) * 3.2)(F, D) * F ** (-0.5))}
    _B_in = _B_setup()
    return {'A_x': _A_in['x'], 'A_k_cache': _A_in['k_cache'], 'A_v_cache': _A_in['v_cache'], 'A_wq': _A_in['wq'], 'A_wo': _A_in['wo'], 'B_x': _B_in['x'], 'B_w1': _B_in['w1'], 'B_w2': _B_in['w2'], 'c': _w((lambda *s: torch.randn(*s, dtype=torch.bfloat16) * 0.2)(1, 576) * 8.0), 'w_mod': _w((lambda *s: torch.randn(*s, dtype=torch.bfloat16) * 0.2)(576, 6 * 576) * 576 ** (-0.5) * 8.0)}

def reference(A_x, A_k_cache, A_v_cache, A_wq, A_wo, B_x, B_w1, B_w2, c, w_mod):

    def _A_ref(x, k_cache, v_cache, wq, wo):
        B, Sq, D = x.shape
        Hq, Dh = (32, 128)
        Hkv = k_cache.shape[1]
        rep = Hq // Hkv
        q = (lambda x, *d: x.permute(*d))((x @ wq).reshape(B, Sq, Hq, Dh), 0, 2, 1, 3)
        k = (lambda x, n, axis: torch.repeat_interleave(x, n, dim=axis))(k_cache, rep, axis=1)
        v = (lambda x, n, axis: torch.repeat_interleave(x, n, dim=axis))(v_cache, rep, axis=1)
        scores = torch.einsum('bhqd,bhkd->bhqk', q, k) * Dh ** (-0.5)
        p = _softmax(scores, axis=-1)
        o = torch.einsum('bhqk,bhkd->bhqd', p, v)
        return (lambda x, *d: x.permute(*d))(o, 0, 2, 1, 3).reshape(B, Sq, Hq * Dh) @ wo

    def _B_ref(x, w1, w2):
        h = torch.einsum('bsd,df->bsf', x, w1)
        h = _silu(h)
        out = torch.einsum('bsf,fd->bsd', h, w2)
        return out
    x = A_x
    mod = (_silu(c) @ w_mod).reshape(c.shape[0], 6, 1, -1)
    sa, ca, ga, sb, cb, gb = (mod[:, 0].clone(), mod[:, 1].clone(), mod[:, 2].clone(), mod[:, 3].clone(), mod[:, 4].clone(), mod[:, 5].clone())
    h = _layernorm(x, axis=-1) * (1 + ca) + sa
    x = x + ga * _A_ref(h, A_k_cache, A_v_cache, A_wq, A_wo)
    h = _layernorm(x, axis=-1) * (1 + cb) + sb
    x = x + gb * _B_ref(h, B_w1, B_w2)
    return x
```

```cpp
#include <hip/hip_runtime.h>
#include <hip/hip_bf16.h>
#include <stdint.h>

#define MIMIC 1

typedef unsigned short u16;
typedef unsigned u32x4 __attribute__((ext_vector_type(4)));
constexpr int D = 576, HQ = 32, HKV = 16, SK = 8192, DH = 128, F = 4608, NMOD = 6 * D;
constexpr int NSPLIT = 32;
constexpr int KEYS = SK / NSPLIT;

__device__ __forceinline__ float bf2f(u16 b) { return __uint_as_float(((unsigned)b) << 16); }
__device__ __forceinline__ float rbf(float f) {
  unsigned u = __float_as_uint(f);
  u += 0x7FFFu + ((u >> 16) & 1u);
  return __uint_as_float(u & 0xFFFF0000u);
}
__device__ __forceinline__ u16 f2bf(float f) {
  unsigned u = __float_as_uint(f);
  u += 0x7FFFu + ((u >> 16) & 1u);
  return (u16)(u >> 16);
}
__device__ __forceinline__ float block_sum(float v, float* red) {
  for (int o = 32; o > 0; o >>= 1) v += __shfl_xor(v, o);
  __syncthreads();
  if ((threadIdx.x & 63) == 0) red[threadIdx.x >> 6] = v;
  __syncthreads();
  float s = 0.f;
  for (int i = 0; i < (int)(blockDim.x >> 6); ++i) s += red[i];
  return s;
}

__device__ __forceinline__ void gemv_cols64(const float* xs, int K, const u16* W, int N, int n0, float* red  , float* outv  ) {
  const int cg = threadIdx.x & 7, kl = threadIdx.x >> 3;
  float acc[8] = {0, 0, 0, 0, 0, 0, 0, 0};
  for (int k = kl; k < K; k += 32) {
    u32x4 w = *reinterpret_cast<const u32x4*>(W + (size_t)k * N + n0 + cg * 8);
    float xv = xs[k];
#pragma unroll
    for (int j = 0; j < 4; ++j) {
      acc[2 * j] = fmaf(xv, __uint_as_float(w[j] << 16), acc[2 * j]);
      acc[2 * j + 1] = fmaf(xv, __uint_as_float(w[j] & 0xFFFF0000u), acc[2 * j + 1]);
    }
  }
#pragma unroll
  for (int j = 0; j < 8; ++j) red[kl * 64 + cg * 8 + j] = acc[j];
  __syncthreads();
  if (threadIdx.x < 64) {
    float s = 0.f;
    for (int i = 0; i < 32; ++i) s += red[i * 64 + threadIdx.x];
    outv[threadIdx.x] = s;
  }
  __syncthreads();
}

__global__ void __launch_bounds__(256) k_mod(const u16* c, const u16* wmod, float* mod) {
  __shared__ float xs[D]; __shared__ float red[32 * 64]; __shared__ float o[64];
  for (int i = threadIdx.x; i < D; i += 256) { float v = bf2f(c[i]); xs[i] = rbf(v / (1.f + __expf(-v))); }
  __syncthreads();
  gemv_cols64(xs, D, wmod, NMOD, blockIdx.x * 64, red, o);
  if (threadIdx.x < 64) mod[blockIdx.x * 64 + threadIdx.x] = rbf(o[threadIdx.x]);
}

__device__ __forceinline__ void ln_mod(const float* xin  , const float* shift, const float* scale, float* h  , float* red) {
  float s = 0.f;
  for (int i = threadIdx.x; i < D; i += blockDim.x) s += xin[i];
  float mean = block_sum(s, red) / D;
  float v = 0.f;
  for (int i = threadIdx.x; i < D; i += blockDim.x) { float d = xin[i] - mean; v += d * d; }
  float var = rbf(block_sum(v, red) / D);
  float m = rbf(mean);
  float den = rbf(sqrtf(rbf(var + 1e-5f)));
  for (int i = threadIdx.x; i < D; i += blockDim.x) {
    float t = rbf(rbf(xin[i] - m) / den);
    h[i] = rbf(rbf(t * rbf(1.f + scale[i])) + shift[i]);
  }
  __syncthreads();
}

__global__ void __launch_bounds__(256) k_q(const u16* x, const float* mod, const u16* wq, float* q) {
  __shared__ float xs[D]; __shared__ float h[D]; __shared__ float red[32 * 64]; __shared__ float o[64];
  for (int i = threadIdx.x; i < D; i += 256) xs[i] = bf2f(x[i]);
  __syncthreads();
  ln_mod(xs, mod, mod + D, h, red);
  gemv_cols64(h, D, wq, HQ * DH, blockIdx.x * 64, red, o);
  if (threadIdx.x < 64) q[blockIdx.x * 64 + threadIdx.x] = rbf(o[threadIdx.x]);
}

__global__ void __launch_bounds__(256) k_attn(const float* q, const u16* K, const u16* V, float* pml, float* po) {
  __shared__ float sc[2][KEYS]; __shared__ float red[16][2][DH]; __shared__ float r2[8];
  const int s = blockIdx.x, h = blockIdx.y, t = threadIdx.x, c = t & 15, r = t >> 4;
  float qv[2][8];
#pragma unroll
  for (int g = 0; g < 2; ++g)
#pragma unroll
    for (int j = 0; j < 8; ++j) qv[g][j] = q[(h * 2 + g) * DH + c * 8 + j];
  const u16* kb = K + ((size_t)h * SK + s * KEYS) * DH + c * 8;
  const u16* vb = V + ((size_t)h * SK + s * KEYS) * DH + c * 8;
  u32x4 kr[KEYS / 16], vr[KEYS / 16];
#pragma unroll
  for (int i = 0; i < KEYS / 16; ++i) kr[i] = __builtin_nontemporal_load(reinterpret_cast<const u32x4*>(kb + (size_t)(r + 16 * i) * DH));
#pragma unroll
  for (int i = 0; i < KEYS / 16; ++i) vr[i] = __builtin_nontemporal_load(reinterpret_cast<const u32x4*>(vb + (size_t)(r + 16 * i) * DH));
#pragma unroll
  for (int i = 0; i < KEYS / 16; ++i) {
    float d0 = 0.f, d1 = 0.f;
#pragma unroll
    for (int j = 0; j < 4; ++j) {
      float a = __uint_as_float(kr[i][j] << 16), b = __uint_as_float(kr[i][j] & 0xFFFF0000u);
      d0 = fmaf(a, qv[0][2 * j], d0); d0 = fmaf(b, qv[0][2 * j + 1], d0);
      d1 = fmaf(a, qv[1][2 * j], d1); d1 = fmaf(b, qv[1][2 * j + 1], d1);
    }
    for (int o = 8; o > 0; o >>= 1) { d0 += __shfl_xor(d0, o); d1 += __shfl_xor(d1, o); }
    if (c == 0) { sc[0][r + 16 * i] = rbf(rbf(d0) * 0.08838834764831845f); sc[1][r + 16 * i] = rbf(rbf(d1) * 0.08838834764831845f); }
  }
  __syncthreads();
  float m0 = sc[0][t], m1 = sc[1][t];
  float s0 = m0, s1 = m1;
  for (int o = 32; o > 0; o >>= 1) { m0 = fmaxf(m0, __shfl_xor(m0, o)); m1 = fmaxf(m1, __shfl_xor(m1, o)); }
  if ((t & 63) == 0) { r2[t >> 6] = m0; r2[4 + (t >> 6)] = m1; }
  __syncthreads();
  m0 = fmaxf(fmaxf(r2[0], r2[1]), fmaxf(r2[2], r2[3]));
  m1 = fmaxf(fmaxf(r2[4], r2[5]), fmaxf(r2[6], r2[7]));
  float p0 = __expf(s0 - m0), p1 = __expf(s1 - m1);
  __syncthreads();
  sc[0][t] = p0; sc[1][t] = p1;
  for (int o = 32; o > 0; o >>= 1) { p0 += __shfl_xor(p0, o); p1 += __shfl_xor(p1, o); }
  if ((t & 63) == 0) { r2[t >> 6] = p0; r2[4 + (t >> 6)] = p1; }
  __syncthreads();
  float l0 = r2[0] + r2[1] + r2[2] + r2[3], l1 = r2[4] + r2[5] + r2[6] + r2[7];
  float a0[8] = {0, 0, 0, 0, 0, 0, 0, 0}, a1[8] = {0, 0, 0, 0, 0, 0, 0, 0};
#pragma unroll
  for (int i = 0; i < KEYS / 16; ++i) {
    float w0 = sc[0][r + 16 * i], w1 = sc[1][r + 16 * i];
#pragma unroll
    for (int j = 0; j < 4; ++j) {
      float a = __uint_as_float(vr[i][j] << 16), b = __uint_as_float(vr[i][j] & 0xFFFF0000u);
      a0[2 * j] = fmaf(w0, a, a0[2 * j]); a0[2 * j + 1] = fmaf(w0, b, a0[2 * j + 1]);
      a1[2 * j] = fmaf(w1, a, a1[2 * j]); a1[2 * j + 1] = fmaf(w1, b, a1[2 * j + 1]);
    }
  }
#pragma unroll
  for (int j = 0; j < 8; ++j) { red[r][0][c * 8 + j] = a0[j]; red[r][1][c * 8 + j] = a1[j]; }
  __syncthreads();
  { const int g = t >> 7, d = t & 127; float sum = 0.f;
    for (int i = 0; i < 16; ++i) sum += red[i][g][d];
    po[((size_t)(h * 2 + g) * NSPLIT + s) * DH + d] = sum;
    if (d == 0) { pml[((h * 2 + g) * NSPLIT + s) * 2] = g ? m1 : m0; pml[((h * 2 + g) * NSPLIT + s) * 2 + 1] = g ? l1 : l0; } }
}

__global__ void __launch_bounds__(576) k_comb_wo(const float* pml, const float* po, const u16* wo, float* part) {
  __shared__ float o[DH]; __shared__ float red[8][D];
  const int h = blockIdx.x, t = threadIdx.x;
  if (t < DH) {
    float M = -INFINITY;
    for (int s = 0; s < NSPLIT; ++s) M = fmaxf(M, pml[(h * NSPLIT + s) * 2]);
    float L = 0.f, acc = 0.f;
    for (int s = 0; s < NSPLIT; ++s) { float w = __expf(pml[(h * NSPLIT + s) * 2] - M); L = fmaf(w, pml[(h * NSPLIT + s) * 2 + 1], L); acc = fmaf(w, po[((size_t)h * NSPLIT + s) * DH + t], acc); }
    o[t] = rbf(acc / L);
  }
  __syncthreads();
  const int cg = t % 72, kl = t / 72;
  float acc[8] = {0, 0, 0, 0, 0, 0, 0, 0};
  for (int k = kl; k < DH; k += 8) {
    u32x4 w = *reinterpret_cast<const u32x4*>(wo + (size_t)(h * DH + k) * D + cg * 8);
    float xv = o[k];
#pragma unroll
    for (int j = 0; j < 4; ++j) { acc[2 * j] = fmaf(xv, __uint_as_float(w[j] << 16), acc[2 * j]); acc[2 * j + 1] = fmaf(xv, __uint_as_float(w[j] & 0xFFFF0000u), acc[2 * j + 1]); }
  }
#pragma unroll
  for (int j = 0; j < 8; ++j) red[kl][cg * 8 + j] = acc[j];
  __syncthreads();
  float s = 0.f;
  for (int i = 0; i < 8; ++i) s += red[i][t];
  part[h * D + t] = s;
}

__global__ void __launch_bounds__(256) k_mlp(const u16* x, const float* mod, const float* part, const u16* w1, const u16* w2, float* x1g, float* part2) {
  __shared__ float xs[D]; __shared__ float h[D]; __shared__ float red[32 * 64]; __shared__ float o[64]; __shared__ float red2[3][D];
  for (int i = threadIdx.x; i < D; i += 256) {
    float s = 0.f;
    for (int k = 0; k < HQ; ++k) s += part[k * D + i];
    float v = rbf(bf2f(x[i]) + rbf(mod[2 * D + i] * rbf(s)));
    xs[i] = v;
    if (blockIdx.x == 0) x1g[i] = v;
  }
  __syncthreads();
  ln_mod(xs, mod + 3 * D, mod + 4 * D, h, red);
  gemv_cols64(h, D, w1, F, blockIdx.x * 64, red, o);
  if (threadIdx.x < 64) { float v = rbf(o[threadIdx.x]); o[threadIdx.x] = rbf(v / (1.f + __expf(-v))); }
  __syncthreads();
  const int t = threadIdx.x;
  if (t < 216) {
    const int cg = t % 72, kl = t / 72;
    float acc[8] = {0, 0, 0, 0, 0, 0, 0, 0};
    for (int k = kl; k < 64; k += 3) {
      u32x4 w = *reinterpret_cast<const u32x4*>(w2 + (size_t)(blockIdx.x * 64 + k) * D + cg * 8);
      float xv = o[k];
#pragma unroll
      for (int j = 0; j < 4; ++j) { acc[2 * j] = fmaf(xv, __uint_as_float(w[j] << 16), acc[2 * j]); acc[2 * j + 1] = fmaf(xv, __uint_as_float(w[j] & 0xFFFF0000u), acc[2 * j + 1]); }
    }
#pragma unroll
    for (int j = 0; j < 8; ++j) red2[kl][cg * 8 + j] = acc[j];
  }
  __syncthreads();
  for (int i = t; i < D; i += 256) part2[blockIdx.x * D + i] = red2[0][i] + red2[1][i] + red2[2][i];
}

__global__ void __launch_bounds__(64) k_fin(const float* mod, const float* x1g, const float* part2, u16* out) {
  const int i = blockIdx.x * 64 + threadIdx.x;
  float s = 0.f;
  for (int k = 0; k < F / 64; ++k) s += part2[k * D + i];
  out[i] = f2bf(x1g[i] + rbf(mod[5 * D + i] * rbf(s)));
}

extern "C" void kernel_launch(void* const* d_in, const int* in_sizes, int n_in, void* d_out, int out_size, void* d_ws, size_t ws_size, hipStream_t stream) {
  const u16* x = (const u16*)d_in[0]; const u16* K = (const u16*)d_in[1]; const u16* V = (const u16*)d_in[2];
  const u16* wq = (const u16*)d_in[3]; const u16* wo = (const u16*)d_in[4];
  const u16* w1 = (const u16*)d_in[6]; const u16* w2 = (const u16*)d_in[7];
  const u16* c = (const u16*)d_in[8]; const u16* wmod = (const u16*)d_in[9];
  float* ws = (float*)d_ws;
  float* mod = ws; float* q = ws + 4096; float* pml = ws + 8192; float* po = ws + 16384;
  float* part = po + HQ * NSPLIT * DH; float* x1g = part + HQ * D; float* part2 = x1g + 1024;
  k_mod<<<NMOD / 64, 256, 0, stream>>>(c, wmod, mod);
  k_q<<<HQ * DH / 64, 256, 0, stream>>>(x, mod, wq, q);
  k_attn<<<dim3(NSPLIT, HKV), 256, 0, stream>>>(q, K, V, pml, po);
  k_comb_wo<<<HQ, 576, 0, stream>>>(pml, po, wo, part);
  k_mlp<<<F / 64, 256, 0, stream>>>(x, mod, part, w1, w2, x1g, part2);
  k_fin<<<D / 64, 64, 0, stream>>>(mod, x1g, part2, (u16*)d_out);
}
```

```cpp
#define NT 1
#define PROBE_MEAS 1
#include <hip/hip_runtime.h>
#include <hip/hip_bf16.h>
#include <stdint.h>

#define MIMIC 1

typedef unsigned short u16;
typedef unsigned u32x4 __attribute__((ext_vector_type(4)));
constexpr int D = 576, HQ = 32, HKV = 16, SK = 8192, DH = 128, F = 4608, NMOD = 6 * D;
constexpr int NSPLIT = 32;
constexpr int KEYS = SK / NSPLIT;

__device__ __forceinline__ float bf2f(u16 b) { return __uint_as_float(((unsigned)b) << 16); }
__device__ __forceinline__ float rbf(float f) {
  unsigned u = __float_as_uint(f);
  u += 0x7FFFu + ((u >> 16) & 1u);
  return __uint_as_float(u & 0xFFFF0000u);
}
__device__ __forceinline__ u16 f2bf(float f) {
  unsigned u = __float_as_uint(f);
  u += 0x7FFFu + ((u >> 16) & 1u);
  return (u16)(u >> 16);
}
__device__ __forceinline__ float block_sum(float v, float* red) {
  for (int o = 32; o > 0; o >>= 1) v += __shfl_xor(v, o);
  __syncthreads();
  if ((threadIdx.x & 63) == 0) red[threadIdx.x >> 6] = v;
  __syncthreads();
  float s = 0.f;
  for (int i = 0; i < (int)(blockDim.x >> 6); ++i) s += red[i];
  return s;
}

__device__ __forceinline__ void gemv_cols64(const float* xs, int K, const u16* W, int N, int n0, float* red  , float* outv  ) {
  const int cg = threadIdx.x & 7, kl = threadIdx.x >> 3;
  float acc[8] = {0, 0, 0, 0, 0, 0, 0, 0};
  for (int k = kl; k < K; k += 32) {
    u32x4 w = *reinterpret_cast<const u32x4*>(W + (size_t)k * N + n0 + cg * 8);
    float xv = xs[k];
#pragma unroll
    for (int j = 0; j < 4; ++j) {
      acc[2 * j] = fmaf(xv, __uint_as_float(w[j] << 16), acc[2 * j]);
      acc[2 * j + 1] = fmaf(xv, __uint_as_float(w[j] & 0xFFFF0000u), acc[2 * j + 1]);
    }
  }
#pragma unroll
  for (int j = 0; j < 8; ++j) red[kl * 64 + cg * 8 + j] = acc[j];
  __syncthreads();
  if (threadIdx.x < 64) {
    float s = 0.f;
    for (int i = 0; i < 32; ++i) s += red[i * 64 + threadIdx.x];
    outv[threadIdx.x] = s;
  }
  __syncthreads();
}

__global__ void __launch_bounds__(256) k_mod(const u16* c, const u16* wmod, float* mod) {
  __shared__ float xs[D]; __shared__ float red[32 * 64]; __shared__ float o[64];
  for (int i = threadIdx.x; i < D; i += 256) { float v = bf2f(c[i]); xs[i] = rbf(v / (1.f + __expf(-v))); }
  __syncthreads();
  gemv_cols64(xs, D, wmod, NMOD, blockIdx.x * 64, red, o);
  if (threadIdx.x < 64) mod[blockIdx.x * 64 + threadIdx.x] = rbf(o[threadIdx.x]);
}

__device__ __forceinline__ void ln_mod(const float* xin  , const float* shift, const float* scale, float* h  , float* red) {
  float s = 0.f;
  for (int i = threadIdx.x; i < D; i += blockDim.x) s += xin[i];
  float mean = block_sum(s, red) / D;
  float v = 0.f;
  for (int i = threadIdx.x; i < D; i += blockDim.x) { float d = xin[i] - mean; v += d * d; }
  float var = rbf(block_sum(v, red) / D);
  float m = rbf(mean);
  float den = rbf(sqrtf(rbf(var + 1e-5f)));
  for (int i = threadIdx.x; i < D; i += blockDim.x) {
    float t = rbf(rbf(xin[i] - m) / den);
    h[i] = rbf(rbf(t * rbf(1.f + scale[i])) + shift[i]);
  }
  __syncthreads();
}

__global__ void __launch_bounds__(256) k_q(const u16* x, const float* mod, const u16* wq, float* q) {
  __shared__ float xs[D]; __shared__ float h[D]; __shared__ float red[32 * 64]; __shared__ float o[64];
  for (int i = threadIdx.x; i < D; i += 256) xs[i] = bf2f(x[i]);
  __syncthreads();
  ln_mod(xs, mod, mod + D, h, red);
  gemv_cols64(h, D, wq, HQ * DH, blockIdx.x * 64, red, o);
  if (threadIdx.x < 64) q[blockIdx.x * 64 + threadIdx.x] = rbf(o[threadIdx.x]);
}

__global__ void __launch_bounds__(256) k_attn(const float* q, const u16* K, const u16* V, float* pml, float* po) {
  __shared__ float sc[2][KEYS]; __shared__ float red[16][2][DH]; __shared__ float r2[8];
  const int s = blockIdx.x, h = blockIdx.y, t = threadIdx.x, c = t & 15, r = t >> 4;
  float qv[2][8];
#pragma unroll
  for (int g = 0; g < 2; ++g)
#pragma unroll
    for (int j = 0; j < 8; ++j) qv[g][j] = q[(h * 2 + g) * DH + c * 8 + j];
  const u16* kb = K + ((size_t)h * SK + s * KEYS) * DH + c * 8;
  const u16* vb = V + ((size_t)h * SK + s * KEYS) * DH + c * 8;
  u32x4 kr[KEYS / 16], vr[KEYS / 16];
#pragma unroll
  for (int i = 0; i < KEYS / 16; ++i) kr[i] = __builtin_nontemporal_load(reinterpret_cast<const u32x4*>(kb + (size_t)(r + 16 * i) * DH));
#pragma unroll
  for (int i = 0; i < KEYS / 16; ++i) vr[i] = __builtin_nontemporal_load(reinterpret_cast<const u32x4*>(vb + (size_t)(r + 16 * i) * DH));
#pragma unroll
  for (int i = 0; i < KEYS / 16; ++i) {
    float d0 = 0.f, d1 = 0.f;
#pragma unroll
    for (int j = 0; j < 4; ++j) {
      float a = __uint_as_float(kr[i][j] << 16), b = __uint_as_float(kr[i][j] & 0xFFFF0000u);
      d0 = fmaf(a, qv[0][2 * j], d0); d0 = fmaf(b, qv[0][2 * j + 1], d0);
      d1 = fmaf(a, qv[1][2 * j], d1); d1 = fmaf(b, qv[1][2 * j + 1], d1);
    }
    for (int o = 8; o > 0; o >>= 1) { d0 += __shfl_xor(d0, o); d1 += __shfl_xor(d1, o); }
    if (c == 0) { sc[0][r + 16 * i] = rbf(rbf(d0) * 0.08838834764831845f); sc[1][r + 16 * i] = rbf(rbf(d1) * 0.08838834764831845f); }
  }
  __syncthreads();
  float m0 = sc[0][t], m1 = sc[1][t];
  float s0 = m0, s1 = m1;
  for (int o = 32; o > 0; o >>= 1) { m0 = fmaxf(m0, __shfl_xor(m0, o)); m1 = fmaxf(m1, __shfl_xor(m1, o)); }
  if ((t & 63) == 0) { r2[t >> 6] = m0; r2[4 + (t >> 6)] = m1; }
  __syncthreads();
  m0 = fmaxf(fmaxf(r2[0], r2[1]), fmaxf(r2[2], r2[3]));
  m1 = fmaxf(fmaxf(r2[4], r2[5]), fmaxf(r2[6], r2[7]));
  float p0 = __expf(s0 - m0), p1 = __expf(s1 - m1);
  __syncthreads();
  sc[0][t] = p0; sc[1][t] = p1;
  for (int o = 32; o > 0; o >>= 1) { p0 += __shfl_xor(p0, o); p1 += __shfl_xor(p1, o); }
  if ((t & 63) == 0) { r2[t >> 6] = p0; r2[4 + (t >> 6)] = p1; }
  __syncthreads();
  float l0 = r2[0] + r2[1] + r2[2] + r2[3], l1 = r2[4] + r2[5] + r2[6] + r2[7];
  float a0[8] = {0, 0, 0, 0, 0, 0, 0, 0}, a1[8] = {0, 0, 0, 0, 0, 0, 0, 0};
#pragma unroll
  for (int i = 0; i < KEYS / 16; ++i) {
    float w0 = sc[0][r + 16 * i], w1 = sc[1][r + 16 * i];
#pragma unroll
    for (int j = 0; j < 4; ++j) {
      float a = __uint_as_float(vr[i][j] << 16), b = __uint_as_float(vr[i][j] & 0xFFFF0000u);
      a0[2 * j] = fmaf(w0, a, a0[2 * j]); a0[2 * j + 1] = fmaf(w0, b, a0[2 * j + 1]);
      a1[2 * j] = fmaf(w1, a, a1[2 * j]); a1[2 * j + 1] = fmaf(w1, b, a1[2 * j + 1]);
    }
  }
#pragma unroll
  for (int j = 0; j < 8; ++j) { red[r][0][c * 8 + j] = a0[j]; red[r][1][c * 8 + j] = a1[j]; }
  __syncthreads();
  { const int g = t >> 7, d = t & 127; float sum = 0.f;
    for (int i = 0; i < 16; ++i) sum += red[i][g][d];
    po[((size_t)(h * 2 + g) * NSPLIT + s) * DH + d] = sum;
    if (d == 0) { pml[((h * 2 + g) * NSPLIT + s) * 2] = g ? m1 : m0; pml[((h * 2 + g) * NSPLIT + s) * 2 + 1] = g ? l1 : l0; } }
}

__global__ void __launch_bounds__(576) k_comb_wo(const float* pml, const float* po, const u16* wo, float* part) {
  __shared__ float o[DH]; __shared__ float red[8][D];
  const int h = blockIdx.x, t = threadIdx.x;
  if (t < DH) {
    float M = -INFINITY;
    for (int s = 0; s < NSPLIT; ++s) M = fmaxf(M, pml[(h * NSPLIT + s) * 2]);
    float L = 0.f, acc = 0.f;
    for (int s = 0; s < NSPLIT; ++s) { float w = __expf(pml[(h * NSPLIT + s) * 2] - M); L = fmaf(w, pml[(h * NSPLIT + s) * 2 + 1], L); acc = fmaf(w, po[((size_t)h * NSPLIT + s) * DH + t], acc); }
    o[t] = rbf(acc / L);
  }
  __syncthreads();
  const int cg = t % 72, kl = t / 72;
  float acc[8] = {0, 0, 0, 0, 0, 0, 0, 0};
  for (int k = kl; k < DH; k += 8) {
    u32x4 w = *reinterpret_cast<const u32x4*>(wo + (size_t)(h * DH + k) * D + cg * 8);
    float xv = o[k];
#pragma unroll
    for (int j = 0; j < 4; ++j) { acc[2 * j] = fmaf(xv, __uint_as_float(w[j] << 16), acc[2 * j]); acc[2 * j + 1] = fmaf(xv, __uint_as_float(w[j] & 0xFFFF0000u), acc[2 * j + 1]); }
  }
#pragma unroll
  for (int j = 0; j < 8; ++j) red[kl][cg * 8 + j] = acc[j];
  __syncthreads();
  float s = 0.f;
  for (int i = 0; i < 8; ++i) s += red[i][t];
  part[h * D + t] = s;
}

__global__ void __launch_bounds__(256) k_mlp(const u16* x, const float* mod, const float* part, const u16* w1, const u16* w2, float* x1g, float* part2) {
  __shared__ float xs[D]; __shared__ float h[D]; __shared__ float red[32 * 64]; __shared__ float o[64]; __shared__ float red2[3][D];
  for (int i = threadIdx.x; i < D; i += 256) {
    float s = 0.f;
    for (int k = 0; k < HQ; ++k) s += part[k * D + i];
    float v = rbf(bf2f(x[i]) + rbf(mod[2 * D + i] * rbf(s)));
    xs[i] = v;
    if (blockIdx.x == 0) x1g[i] = v;
  }
  __syncthreads();
  ln_mod(xs, mod + 3 * D, mod + 4 * D, h, red);
  gemv_cols64(h, D, w1, F, blockIdx.x * 64, red, o);
  if (threadIdx.x < 64) { float v = rbf(o[threadIdx.x]); o[threadIdx.x] = rbf(v / (1.f + __expf(-v))); }
  __syncthreads();
  const int t = threadIdx.x;
  if (t < 216) {
    const int cg = t % 72, kl = t / 72;
    float acc[8] = {0, 0, 0, 0, 0, 0, 0, 0};
    for (int k = kl; k < 64; k += 3) {
      u32x4 w = *reinterpret_cast<const u32x4*>(w2 + (size_t)(blockIdx.x * 64 + k) * D + cg * 8);
      float xv = o[k];
#pragma unroll
      for (int j = 0; j < 4; ++j) { acc[2 * j] = fmaf(xv, __uint_as_float(w[j] << 16), acc[2 * j]); acc[2 * j + 1] = fmaf(xv, __uint_as_float(w[j] & 0xFFFF0000u), acc[2 * j + 1]); }
    }
#pragma unroll
    for (int j = 0; j < 8; ++j) red2[kl][cg * 8 + j] = acc[j];
  }
  __syncthreads();
  for (int i = t; i < D; i += 256) part2[blockIdx.x * D + i] = red2[0][i] + red2[1][i] + red2[2][i];
}

__global__ void __launch_bounds__(64) k_fin(const float* mod, const float* x1g, const float* part2, u16* out) {
  const int i = blockIdx.x * 64 + threadIdx.x;
  float s = 0.f;
  for (int k = 0; k < F / 64; ++k) s += part2[k * D + i];
  out[i] = f2bf(x1g[i] + rbf(mod[5 * D + i] * rbf(s)));
}

__device__ __forceinline__ unsigned long long rt() { return __builtin_amdgcn_s_memrealtime(); }
__global__ void __launch_bounds__(256) k_spin(const unsigned long long* st, int nwg, int meas) {
  __shared__ unsigned long long a[256][4];
  const int t = threadIdx.x;
  unsigned long long mn0 = ~0ull, mx0 = 0, mx1 = 0, mxd = 0, mnd = ~0ull, mx2 = 0;
  for (int w = 0; w < nwg; ++w) {
    unsigned long long s0 = st[w * 8], s1 = st[w * 8 + 1], s2 = st[w * 8 + 2];
    mn0 = s0 < mn0 ? s0 : mn0; mx0 = s0 > mx0 ? s0 : mx0; mx1 = s1 > mx1 ? s1 : mx1; mx2 = s2 > mx2 ? s2 : mx2;
    unsigned long long d = s2 - s0; mxd = d > mxd ? d : mxd; mnd = d < mnd ? d : mnd;
  }
  unsigned long long m;
  if (meas == 0) m = mx1 - mn0;
  else if (meas == 1) m = mx0 - mn0;
  else if (meas == 2) m = mxd;
  else if (meas == 3) m = mnd;
  else m = mx2 - mn0;
  if (m > 10000) m = 10000;
  const unsigned long long target = mn0 + 30000 + 50 * m;
  while (rt() < target) __builtin_amdgcn_s_sleep(8);
}
struct P { const u32x4* p[7]; unsigned n[7]; unsigned pad; unsigned long long* st; unsigned* sink; };
#define NTH 512
#define UNR 8
__device__ __forceinline__ u32x4 ldx(const u32x4* p) {
  return __builtin_nontemporal_load(p);
}
__global__ void __launch_bounds__(NTH) k_stream(P a) {
  unsigned long long t0 = rt();
  u32x4 acc = {0,0,0,0};
  const int tid = blockIdx.x * NTH + threadIdx.x;
  const int nthr = 256 * NTH;
  unsigned long long t2 = 0;
#pragma unroll
  for (int b = 0; b < 7; ++b) {
    const u32x4* p = a.p[b]; const unsigned n = a.n[b];
    unsigned i = tid;
    for (; i + (UNR - 1) * nthr < n; i += UNR * nthr) {
      u32x4 v[UNR];
#pragma unroll
      for (int j = 0; j < UNR; ++j) v[j] = ldx(p + i + j * nthr);
#pragma unroll
      for (int j = 0; j < UNR; ++j) acc ^= v[j];
    }
    for (; i < n; i += nthr) acc ^= ldx(p + i);
    if (b == 0) { asm volatile("s_waitcnt vmcnt(0)" :: "v"(acc) : "memory"); t2 = rt(); }
  }
  unsigned r = acc[0] ^ acc[1] ^ acc[2] ^ acc[3];
  if (r == 0x12345678u) a.sink[tid] = r;
  __syncthreads();
  unsigned long long t1 = rt();
  if (threadIdx.x == 0) { a.st[blockIdx.x * 8] = t0; a.st[blockIdx.x * 8 + 1] = t1; a.st[blockIdx.x * 8 + 2] = t2; }
}
#define PROBE_NWG 256
#define PROBE_LAUNCH do { P a; const int idx[7] = {9, 3, 1, 2, 4, 6, 7}; \
  for (int i = 0; i < 7; ++i) { a.p[i] = (const u32x4*)d_in[idx[i]]; a.n[i] = in_sizes[idx[i]] / 8; } \
  a.pad = 0; a.st = (unsigned long long*)((char*)d_ws + (64 << 20)); a.sink = (unsigned*)((char*)d_ws + (128 << 20)); \
  k_stream<<<256, NTH, 0, stream>>>(a); } while (0)
extern "C" void kernel_launch(void* const* d_in, const int* in_sizes, int n_in, void* d_out, int out_size, void* d_ws, size_t ws_size, hipStream_t stream) {
  const u16* x = (const u16*)d_in[0]; const u16* K = (const u16*)d_in[1]; const u16* V = (const u16*)d_in[2];
  const u16* wq = (const u16*)d_in[3]; const u16* wo = (const u16*)d_in[4];
  const u16* w1 = (const u16*)d_in[6]; const u16* w2 = (const u16*)d_in[7];
  const u16* c = (const u16*)d_in[8]; const u16* wmod = (const u16*)d_in[9];
  float* ws = (float*)d_ws;
  float* mod = ws; float* q = ws + 4096; float* pml = ws + 8192; float* po = ws + 16384;
  float* part = po + HQ * NSPLIT * DH; float* x1g = part + HQ * D; float* part2 = x1g + 1024;
  PROBE_LAUNCH;
  k_mod<<<NMOD / 64, 256, 0, stream>>>(c, wmod, mod);
  k_q<<<HQ * DH / 64, 256, 0, stream>>>(x, mod, wq, q);
  k_attn<<<dim3(NSPLIT, HKV), 256, 0, stream>>>(q, K, V, pml, po);
  k_comb_wo<<<HQ, 576, 0, stream>>>(pml, po, wo, part);
  k_mlp<<<F / 64, 256, 0, stream>>>(x, mod, part, w1, w2, x1g, part2);
  k_fin<<<D / 64, 64, 0, stream>>>(mod, x1g, part2, (u16*)d_out);
  k_spin<<<1, 256, 0, stream>>>((const unsigned long long*)((char*)d_ws + (64 << 20)), PROBE_NWG, PROBE_MEAS);
}
```

```cpp
#define NT 1
#define PROBE_MEAS 2
#define NTH 512
#define WIN 8
#include <hip/hip_runtime.h>
#include <hip/hip_bf16.h>
#include <stdint.h>

#define MIMIC 1

typedef unsigned short u16;
typedef unsigned u32x4 __attribute__((ext_vector_type(4)));
constexpr int D = 576, HQ = 32, HKV = 16, SK = 8192, DH = 128, F = 4608, NMOD = 6 * D;
constexpr int NSPLIT = 32;
constexpr int KEYS = SK / NSPLIT;

__device__ __forceinline__ float bf2f(u16 b) { return __uint_as_float(((unsigned)b) << 16); }
__device__ __forceinline__ float rbf(float f) {
  unsigned u = __float_as_uint(f);
  u += 0x7FFFu + ((u >> 16) & 1u);
  return __uint_as_float(u & 0xFFFF0000u);
}
__device__ __forceinline__ u16 f2bf(float f) {
  unsigned u = __float_as_uint(f);
  u += 0x7FFFu + ((u >> 16) & 1u);
  return (u16)(u >> 16);
}
__device__ __forceinline__ float block_sum(float v, float* red) {
  for (int o = 32; o > 0; o >>= 1) v += __shfl_xor(v, o);
  __syncthreads();
  if ((threadIdx.x & 63) == 0) red[threadIdx.x >> 6] = v;
  __syncthreads();
  float s = 0.f;
  for (int i = 0; i < (int)(blockDim.x >> 6); ++i) s += red[i];
  return s;
}

__device__ __forceinline__ void gemv_cols64(const float* xs, int K, const u16* W, int N, int n0, float* red  , float* outv  ) {
  const int cg = threadIdx.x & 7, kl = threadIdx.x >> 3;
  float acc[8] = {0, 0, 0, 0, 0, 0, 0, 0};
  for (int k = kl; k < K; k += 32) {
    u32x4 w = *reinterpret_cast<const u32x4*>(W + (size_t)k * N + n0 + cg * 8);
    float xv = xs[k];
#pragma unroll
    for (int j = 0; j < 4; ++j) {
      acc[2 * j] = fmaf(xv, __uint_as_float(w[j] << 16), acc[2 * j]);
      acc[2 * j + 1] = fmaf(xv, __uint_as_float(w[j] & 0xFFFF0000u), acc[2 * j + 1]);
    }
  }
#pragma unroll
  for (int j = 0; j < 8; ++j) red[kl * 64 + cg * 8 + j] = acc[j];
  __syncthreads();
  if (threadIdx.x < 64) {
    float s = 0.f;
    for (int i = 0; i < 32; ++i) s += red[i * 64 + threadIdx.x];
    outv[threadIdx.x] = s;
  }
  __syncthreads();
}

__global__ void __launch_bounds__(256) k_mod(const u16* c, const u16* wmod, float* mod) {
  __shared__ float xs[D]; __shared__ float red[32 * 64]; __shared__ float o[64];
  for (int i = threadIdx.x; i < D; i += 256) { float v = bf2f(c[i]); xs[i] = rbf(v / (1.f + __expf(-v))); }
  __syncthreads();
  gemv_cols64(xs, D, wmod, NMOD, blockIdx.x * 64, red, o);
  if (threadIdx.x < 64) mod[blockIdx.x * 64 + threadIdx.x] = rbf(o[threadIdx.x]);
}

__device__ __forceinline__ void ln_mod(const float* xin  , const float* shift, const float* scale, float* h  , float* red) {
  float s = 0.f;
  for (int i = threadIdx.x; i < D; i += blockDim.x) s += xin[i];
  float mean = block_sum(s, red) / D;
  float v = 0.f;
  for (int i = threadIdx.x; i < D; i += blockDim.x) { float d = xin[i] - mean; v += d * d; }
  float var = rbf(block_sum(v, red) / D);
  float m = rbf(mean);
  float den = rbf(sqrtf(rbf(var + 1e-5f)));
  for (int i = threadIdx.x; i < D; i += blockDim.x) {
    float t = rbf(rbf(xin[i] - m) / den);
    h[i] = rbf(rbf(t * rbf(1.f + scale[i])) + shift[i]);
  }
  __syncthreads();
}

__global__ void __launch_bounds__(256) k_q(const u16* x, const float* mod, const u16* wq, float* q) {
  __shared__ float xs[D]; __shared__ float h[D]; __shared__ float red[32 * 64]; __shared__ float o[64];
  for (int i = threadIdx.x; i < D; i += 256) xs[i] = bf2f(x[i]);
  __syncthreads();
  ln_mod(xs, mod, mod + D, h, red);
  gemv_cols64(h, D, wq, HQ * DH, blockIdx.x * 64, red, o);
  if (threadIdx.x < 64) q[blockIdx.x * 64 + threadIdx.x] = rbf(o[threadIdx.x]);
}

__global__ void __launch_bounds__(256) k_attn(const float* q, const u16* K, const u16* V, float* pml, float* po) {
  __shared__ float sc[2][KEYS]; __shared__ float red[16][2][DH]; __shared__ float r2[8];
  const int s = blockIdx.x, h = blockIdx.y, t = threadIdx.x, c = t & 15, r = t >> 4;
  float qv[2][8];
#pragma unroll
  for (int g = 0; g < 2; ++g)
#pragma unroll
    for (int j = 0; j < 8; ++j) qv[g][j] = q[(h * 2 + g) * DH + c * 8 + j];
  const u16* kb = K + ((size_t)h * SK + s * KEYS) * DH + c * 8;
  const u16* vb = V + ((size_t)h * SK + s * KEYS) * DH + c * 8;
  u32x4 kr[KEYS / 16], vr[KEYS / 16];
#pragma unroll
  for (int i = 0; i < KEYS / 16; ++i) kr[i] = __builtin_nontemporal_load(reinterpret_cast<const u32x4*>(kb + (size_t)(r + 16 * i) * DH));
#pragma unroll
  for (int i = 0; i < KEYS / 16; ++i) vr[i] = __builtin_nontemporal_load(reinterpret_cast<const u32x4*>(vb + (size_t)(r + 16 * i) * DH));
#pragma unroll
  for (int i = 0; i < KEYS / 16; ++i) {
    float d0 = 0.f, d1 = 0.f;
#pragma unroll
    for (int j = 0; j < 4; ++j) {
      float a = __uint_as_float(kr[i][j] << 16), b = __uint_as_float(kr[i][j] & 0xFFFF0000u);
      d0 = fmaf(a, qv[0][2 * j], d0); d0 = fmaf(b, qv[0][2 * j + 1], d0);
      d1 = fmaf(a, qv[1][2 * j], d1); d1 = fmaf(b, qv[1][2 * j + 1], d1);
    }
    for (int o = 8; o > 0; o >>= 1) { d0 += __shfl_xor(d0, o); d1 += __shfl_xor(d1, o); }
    if (c == 0) { sc[0][r + 16 * i] = rbf(rbf(d0) * 0.08838834764831845f); sc[1][r + 16 * i] = rbf(rbf(d1) * 0.08838834764831845f); }
  }
  __syncthreads();
  float m0 = sc[0][t], m1 = sc[1][t];
  float s0 = m0, s1 = m1;
  for (int o = 32; o > 0; o >>= 1) { m0 = fmaxf(m0, __shfl_xor(m0, o)); m1 = fmaxf(m1, __shfl_xor(m1, o)); }
  if ((t & 63) == 0) { r2[t >> 6] = m0; r2[4 + (t >> 6)] = m1; }
  __syncthreads();
  m0 = fmaxf(fmaxf(r2[0], r2[1]), fmaxf(r2[2], r2[3]));
  m1 = fmaxf(fmaxf(r2[4], r2[5]), fmaxf(r2[6], r2[7]));
  float p0 = __expf(s0 - m0), p1 = __expf(s1 - m1);
  __syncthreads();
  sc[0][t] = p0; sc[1][t] = p1;
  for (int o = 32; o > 0; o >>= 1) { p0 += __shfl_xor(p0, o); p1 += __shfl_xor(p1, o); }
  if ((t & 63) == 0) { r2[t >> 6] = p0; r2[4 + (t >> 6)] = p1; }
  __syncthreads();
  float l0 = r2[0] + r2[1] + r2[2] + r2[3], l1 = r2[4] + r2[5] + r2[6] + r2[7];
  float a0[8] = {0, 0, 0, 0, 0, 0, 0, 0}, a1[8] = {0, 0, 0, 0, 0, 0, 0, 0};
#pragma unroll
  for (int i = 0; i < KEYS / 16; ++i) {
    float w0 = sc[0][r + 16 * i], w1 = sc[1][r + 16 * i];
#pragma unroll
    for (int j = 0; j < 4; ++j) {
      float a = __uint_as_float(vr[i][j] << 16), b = __uint_as_float(vr[i][j] & 0xFFFF0000u);
      a0[2 * j] = fmaf(w0, a, a0[2 * j]); a0[2 * j + 1] = fmaf(w0, b, a0[2 * j + 1]);
      a1[2 * j] = fmaf(w1, a, a1[2 * j]); a1[2 * j + 1] = fmaf(w1, b, a1[2 * j + 1]);
    }
  }
#pragma unroll
  for (int j = 0; j < 8; ++j) { red[r][0][c * 8 + j] = a0[j]; red[r][1][c * 8 + j] = a1[j]; }
  __syncthreads();
  { const int g = t >> 7, d = t & 127; float sum = 0.f;
    for (int i = 0; i < 16; ++i) sum += red[i][g][d];
    po[((size_t)(h * 2 + g) * NSPLIT + s) * DH + d] = sum;
    if (d == 0) { pml[((h * 2 + g) * NSPLIT + s) * 2] = g ? m1 : m0; pml[((h * 2 + g) * NSPLIT + s) * 2 + 1] = g ? l1 : l0; } }
}

__global__ void __launch_bounds__(576) k_comb_wo(const float* pml, const float* po, const u16* wo, float* part) {
  __shared__ float o[DH]; __shared__ float red[8][D];
  const int h = blockIdx.x, t = threadIdx.x;
  if (t < DH) {
    float M = -INFINITY;
    for (int s = 0; s < NSPLIT; ++s) M = fmaxf(M, pml[(h * NSPLIT + s) * 2]);
    float L = 0.f, acc = 0.f;
    for (int s = 0; s < NSPLIT; ++s) { float w = __expf(pml[(h * NSPLIT + s) * 2] - M); L = fmaf(w, pml[(h * NSPLIT + s) * 2 + 1], L); acc = fmaf(w, po[((size_t)h * NSPLIT + s) * DH + t], acc); }
    o[t] = rbf(acc / L);
  }
  __syncthreads();
  const int cg = t % 72, kl = t / 72;
  float acc[8] = {0, 0, 0, 0, 0, 0, 0, 0};
  for (int k = kl; k < DH; k += 8) {
    u32x4 w = *reinterpret_cast<const u32x4*>(wo + (size_t)(h * DH + k) * D + cg * 8);
    float xv = o[k];
#pragma unroll
    for (int j = 0; j < 4; ++j) { acc[2 * j] = fmaf(xv, __uint_as_float(w[j] << 16), acc[2 * j]); acc[2 * j + 1] = fmaf(xv, __uint_as_float(w[j] & 0xFFFF0000u), acc[2 * j + 1]); }
  }
#pragma unroll
  for (int j = 0; j < 8; ++j) red[kl][cg * 8 + j] = acc[j];
  __syncthreads();
  float s = 0.f;
  for (int i = 0; i < 8; ++i) s += red[i][t];
  part[h * D + t] = s;
}

__global__ void __launch_bounds__(256) k_mlp(const u16* x, const float* mod, const float* part, const u16* w1, const u16* w2, float* x1g, float* part2) {
  __shared__ float xs[D]; __shared__ float h[D]; __shared__ float red[32 * 64]; __shared__ float o[64]; __shared__ float red2[3][D];
  for (int i = threadIdx.x; i < D; i += 256) {
    float s = 0.f;
    for (int k = 0; k < HQ; ++k) s += part[k * D + i];
    float v = rbf(bf2f(x[i]) + rbf(mod[2 * D + i] * rbf(s)));
    xs[i] = v;
    if (blockIdx.x == 0) x1g[i] = v;
  }
  __syncthreads();
  ln_mod(xs, mod + 3 * D, mod + 4 * D, h, red);
  gemv_cols64(h, D, w1, F, blockIdx.x * 64, red, o);
  if (threadIdx.x < 64) { float v = rbf(o[threadIdx.x]); o[threadIdx.x] = rbf(v / (1.f + __expf(-v))); }
  __syncthreads();
  const int t = threadIdx.x;
  if (t < 216) {
    const int cg = t % 72, kl = t / 72;
    float acc[8] = {0, 0, 0, 0, 0, 0, 0, 0};
    for (int k = kl; k < 64; k += 3) {
      u32x4 w = *reinterpret_cast<const u32x4*>(w2 + (size_t)(blockIdx.x * 64 + k) * D + cg * 8);
      float xv = o[k];
#pragma unroll
      for (int j = 0; j < 4; ++j) { acc[2 * j] = fmaf(xv, __uint_as_float(w[j] << 16), acc[2 * j]); acc[2 * j + 1] = fmaf(xv, __uint_as_float(w[j] & 0xFFFF0000u), acc[2 * j + 1]); }
    }
#pragma unroll
    for (int j = 0; j < 8; ++j) red2[kl][cg * 8 + j] = acc[j];
  }
  __syncthreads();
  for (int i = t; i < D; i += 256) part2[blockIdx.x * D + i] = red2[0][i] + red2[1][i] + red2[2][i];
}

__global__ void __launch_bounds__(64) k_fin(const float* mod, const float* x1g, const float* part2, u16* out) {
  const int i = blockIdx.x * 64 + threadIdx.x;
  float s = 0.f;
  for (int k = 0; k < F / 64; ++k) s += part2[k * D + i];
  out[i] = f2bf(x1g[i] + rbf(mod[5 * D + i] * rbf(s)));
}

__device__ __forceinline__ unsigned long long rt() { return __builtin_amdgcn_s_memrealtime(); }
__global__ void __launch_bounds__(256) k_spin(const unsigned long long* st, int nwg, int meas) {
  __shared__ unsigned long long a[256][4];
  const int t = threadIdx.x;
  unsigned long long mn0 = ~0ull, mx0 = 0, mx1 = 0, mxd = 0, mnd = ~0ull, mx2 = 0;
  for (int w = 0; w < nwg; ++w) {
    unsigned long long s0 = st[w * 8], s1 = st[w * 8 + 1], s2 = st[w * 8 + 2];
    mn0 = s0 < mn0 ? s0 : mn0; mx0 = s0 > mx0 ? s0 : mx0; mx1 = s1 > mx1 ? s1 : mx1; mx2 = s2 > mx2 ? s2 : mx2;
    unsigned long long d = s2 - s0; mxd = d > mxd ? d : mxd; mnd = d < mnd ? d : mnd;
  }
  unsigned long long m;
  if (meas == 0) m = mx1 - mn0;
  else if (meas == 1) m = mx0 - mn0;
  else if (meas == 2) m = mxd;
  else if (meas == 3) m = mnd;
  else m = mx2 - mn0;
  if (m > 10000) m = 10000;
  const unsigned long long target = mn0 + 30000 + 50 * m;
  while (rt() < target) __builtin_amdgcn_s_sleep(8);
}
struct P { const u32x4* pk; const u32x4* pv; unsigned long long* st; unsigned* sink; };
__device__ __forceinline__ u32x4 ldx(const u32x4* p) {
  return __builtin_nontemporal_load(p);
}
__global__ void __launch_bounds__(NTH) k_stream(P a) {
  unsigned long long t0 = rt();
  constexpr int NL = 16384 / NTH;
  const u32x4* pk = a.pk + (size_t)blockIdx.x * 8192 + threadIdx.x;
  const u32x4* pv = a.pv + (size_t)blockIdx.x * 8192 + threadIdx.x;
  u32x4 v[WIN];
  u32x4 acc = {0,0,0,0};
  unsigned long long t2 = 0;
#pragma unroll
  for (int j = 0; j < NL + WIN; ++j) {
    if (j >= WIN) { acc ^= v[(j - WIN) % WIN]; if (j == WIN) { asm volatile("" :: "v"(acc) : "memory"); t2 = rt(); } }
    if (j < NL) v[j % WIN] = ldx(j < NL / 2 ? pk + j * NTH : pv + (j - NL / 2) * NTH);
    __builtin_amdgcn_sched_barrier(0);
  }
  unsigned r = acc[0] ^ acc[1] ^ acc[2] ^ acc[3];
  if (r == 0x12345678u) a.sink[threadIdx.x] = r;
  __syncthreads();
  unsigned long long t1 = rt();
  if (threadIdx.x == 0) { a.st[blockIdx.x * 8] = t0; a.st[blockIdx.x * 8 + 1] = t1; a.st[blockIdx.x * 8 + 2] = t2; }
}
#define PROBE_NWG 256
#define PROBE_LAUNCH do { P a; a.pk = (const u32x4*)d_in[1]; a.pv = (const u32x4*)d_in[2]; \
  a.st = (unsigned long long*)((char*)d_ws + (64 << 20)); a.sink = (unsigned*)((char*)d_ws + (128 << 20)); \
  k_stream<<<256, NTH, 0, stream>>>(a); } while (0)
extern "C" void kernel_launch(void* const* d_in, const int* in_sizes, int n_in, void* d_out, int out_size, void* d_ws, size_t ws_size, hipStream_t stream) {
  const u16* x = (const u16*)d_in[0]; const u16* K = (const u16*)d_in[1]; const u16* V = (const u16*)d_in[2];
  const u16* wq = (const u16*)d_in[3]; const u16* wo = (const u16*)d_in[4];
  const u16* w1 = (const u16*)d_in[6]; const u16* w2 = (const u16*)d_in[7];
  const u16* c = (const u16*)d_in[8]; const u16* wmod = (const u16*)d_in[9];
  float* ws = (float*)d_ws;
  float* mod = ws; float* q = ws + 4096; float* pml = ws + 8192; float* po = ws + 16384;
  float* part = po + HQ * NSPLIT * DH; float* x1g = part + HQ * D; float* part2 = x1g + 1024;
  PROBE_LAUNCH;
  k_mod<<<NMOD / 64, 256, 0, stream>>>(c, wmod, mod);
  k_q<<<HQ * DH / 64, 256, 0, stream>>>(x, mod, wq, q);
  k_attn<<<dim3(NSPLIT, HKV), 256, 0, stream>>>(q, K, V, pml, po);
  k_comb_wo<<<HQ, 576, 0, stream>>>(pml, po, wo, part);
  k_mlp<<<F / 64, 256, 0, stream>>>(x, mod, part, w1, w2, x1g, part2);
  k_fin<<<D / 64, 64, 0, stream>>>(mod, x1g, part2, (u16*)d_out);
  k_spin<<<1, 256, 0, stream>>>((const unsigned long long*)((char*)d_ws + (64 << 20)), PROBE_NWG, PROBE_MEAS);
}
```

```cpp
#define STREAM 0
#define PROBE_MEAS 3
#include <hip/hip_runtime.h>
#include <hip/hip_bf16.h>
#include <stdint.h>

#define MIMIC 1

typedef unsigned short u16;
typedef unsigned u32x4 __attribute__((ext_vector_type(4)));
constexpr int D = 576, HQ = 32, HKV = 16, SK = 8192, DH = 128, F = 4608, NMOD = 6 * D;
constexpr int NSPLIT = 32;
constexpr int KEYS = SK / NSPLIT;

__device__ __forceinline__ float bf2f(u16 b) { return __uint_as_float(((unsigned)b) << 16); }
__device__ __forceinline__ float rbf(float f) {
  unsigned u = __float_as_uint(f);
  u += 0x7FFFu + ((u >> 16) & 1u);
  return __uint_as_float(u & 0xFFFF0000u);
}
__device__ __forceinline__ u16 f2bf(float f) {
  unsigned u = __float_as_uint(f);
  u += 0x7FFFu + ((u >> 16) & 1u);
  return (u16)(u >> 16);
}
__device__ __forceinline__ float block_sum(float v, float* red) {
  for (int o = 32; o > 0; o >>= 1) v += __shfl_xor(v, o);
  __syncthreads();
  if ((threadIdx.x & 63) == 0) red[threadIdx.x >> 6] = v;
  __syncthreads();
  float s = 0.f;
  for (int i = 0; i < (int)(blockDim.x >> 6); ++i) s += red[i];
  return s;
}

__device__ __forceinline__ void gemv_cols64(const float* xs, int K, const u16* W, int N, int n0, float* red  , float* outv  ) {
  const int cg = threadIdx.x & 7, kl = threadIdx.x >> 3;
  float acc[8] = {0, 0, 0, 0, 0, 0, 0, 0};
  for (int k = kl; k < K; k += 32) {
    u32x4 w = *reinterpret_cast<const u32x4*>(W + (size_t)k * N + n0 + cg * 8);
    float xv = xs[k];
#pragma unroll
    for (int j = 0; j < 4; ++j) {
      acc[2 * j] = fmaf(xv, __uint_as_float(w[j] << 16), acc[2 * j]);
      acc[2 * j + 1] = fmaf(xv, __uint_as_float(w[j] & 0xFFFF0000u), acc[2 * j + 1]);
    }
  }
#pragma unroll
  for (int j = 0; j < 8; ++j) red[kl * 64 + cg * 8 + j] = acc[j];
  __syncthreads();
  if (threadIdx.x < 64) {
    float s = 0.f;
    for (int i = 0; i < 32; ++i) s += red[i * 64 + threadIdx.x];
    outv[threadIdx.x] = s;
  }
  __syncthreads();
}

__global__ void __launch_bounds__(256) k_mod(const u16* c, const u16* wmod, float* mod) {
  __shared__ float xs[D]; __shared__ float red[32 * 64]; __shared__ float o[64];
  for (int i = threadIdx.x; i < D; i += 256) { float v = bf2f(c[i]); xs[i] = rbf(v / (1.f + __expf(-v))); }
  __syncthreads();
  gemv_cols64(xs, D, wmod, NMOD, blockIdx.x * 64, red, o);
  if (threadIdx.x < 64) mod[blockIdx.x * 64 + threadIdx.x] = rbf(o[threadIdx.x]);
}

__device__ __forceinline__ void ln_mod(const float* xin  , const float* shift, const float* scale, float* h  , float* red) {
  float s = 0.f;
  for (int i = threadIdx.x; i < D; i += blockDim.x) s += xin[i];
  float mean = block_sum(s, red) / D;
  float v = 0.f;
  for (int i = threadIdx.x; i < D; i += blockDim.x) { float d = xin[i] - mean; v += d * d; }
  float var = rbf(block_sum(v, red) / D);
  float m = rbf(mean);
  float den = rbf(sqrtf(rbf(var + 1e-5f)));
  for (int i = threadIdx.x; i < D; i += blockDim.x) {
    float t = rbf(rbf(xin[i] - m) / den);
    h[i] = rbf(rbf(t * rbf(1.f + scale[i])) + shift[i]);
  }
  __syncthreads();
}

__global__ void __launch_bounds__(256) k_q(const u16* x, const float* mod, const u16* wq, float* q) {
  __shared__ float xs[D]; __shared__ float h[D]; __shared__ float red[32 * 64]; __shared__ float o[64];
  for (int i = threadIdx.x; i < D; i += 256) xs[i] = bf2f(x[i]);
  __syncthreads();
  ln_mod(xs, mod, mod + D, h, red);
  gemv_cols64(h, D, wq, HQ * DH, blockIdx.x * 64, red, o);
  if (threadIdx.x < 64) q[blockIdx.x * 64 + threadIdx.x] = rbf(o[threadIdx.x]);
}

__global__ void __launch_bounds__(256) k_attn(const float* q, const u16* K, const u16* V, float* pml, float* po) {
  __shared__ float sc[2][KEYS]; __shared__ float red[16][2][DH]; __shared__ float r2[8];
  const int s = blockIdx.x, h = blockIdx.y, t = threadIdx.x, c = t & 15, r = t >> 4;
  float qv[2][8];
#pragma unroll
  for (int g = 0; g < 2; ++g)
#pragma unroll
    for (int j = 0; j < 8; ++j) qv[g][j] = q[(h * 2 + g) * DH + c * 8 + j];
  const u16* kb = K + ((size_t)h * SK + s * KEYS) * DH + c * 8;
  const u16* vb = V + ((size_t)h * SK + s * KEYS) * DH + c * 8;
  u32x4 kr[KEYS / 16], vr[KEYS / 16];
#pragma unroll
  for (int i = 0; i < KEYS / 16; ++i) kr[i] = __builtin_nontemporal_load(reinterpret_cast<const u32x4*>(kb + (size_t)(r + 16 * i) * DH));
#pragma unroll
  for (int i = 0; i < KEYS / 16; ++i) vr[i] = __builtin_nontemporal_load(reinterpret_cast<const u32x4*>(vb + (size_t)(r + 16 * i) * DH));
#pragma unroll
  for (int i = 0; i < KEYS / 16; ++i) {
    float d0 = 0.f, d1 = 0.f;
#pragma unroll
    for (int j = 0; j < 4; ++j) {
      float a = __uint_as_float(kr[i][j] << 16), b = __uint_as_float(kr[i][j] & 0xFFFF0000u);
      d0 = fmaf(a, qv[0][2 * j], d0); d0 = fmaf(b, qv[0][2 * j + 1], d0);
      d1 = fmaf(a, qv[1][2 * j], d1); d1 = fmaf(b, qv[1][2 * j + 1], d1);
    }
    for (int o = 8; o > 0; o >>= 1) { d0 += __shfl_xor(d0, o); d1 += __shfl_xor(d1, o); }
    if (c == 0) { sc[0][r + 16 * i] = rbf(rbf(d0) * 0.08838834764831845f); sc[1][r + 16 * i] = rbf(rbf(d1) * 0.08838834764831845f); }
  }
  __syncthreads();
  float m0 = sc[0][t], m1 = sc[1][t];
  float s0 = m0, s1 = m1;
  for (int o = 32; o > 0; o >>= 1) { m0 = fmaxf(m0, __shfl_xor(m0, o)); m1 = fmaxf(m1, __shfl_xor(m1, o)); }
  if ((t & 63) == 0) { r2[t >> 6] = m0; r2[4 + (t >> 6)] = m1; }
  __syncthreads();
  m0 = fmaxf(fmaxf(r2[0], r2[1]), fmaxf(r2[2], r2[3]));
  m1 = fmaxf(fmaxf(r2[4], r2[5]), fmaxf(r2[6], r2[7]));
  float p0 = __expf(s0 - m0), p1 = __expf(s1 - m1);
  __syncthreads();
  sc[0][t] = p0; sc[1][t] = p1;
  for (int o = 32; o > 0; o >>= 1) { p0 += __shfl_xor(p0, o); p1 += __shfl_xor(p1, o); }
  if ((t & 63) == 0) { r2[t >> 6] = p0; r2[4 + (t >> 6)] = p1; }
  __syncthreads();
  float l0 = r2[0] + r2[1] + r2[2] + r2[3], l1 = r2[4] + r2[5] + r2[6] + r2[7];
  float a0[8] = {0, 0, 0, 0, 0, 0, 0, 0}, a1[8] = {0, 0, 0, 0, 0, 0, 0, 0};
#pragma unroll
  for (int i = 0; i < KEYS / 16; ++i) {
    float w0 = sc[0][r + 16 * i], w1 = sc[1][r + 16 * i];
#pragma unroll
    for (int j = 0; j < 4; ++j) {
      float a = __uint_as_float(vr[i][j] << 16), b = __uint_as_float(vr[i][j] & 0xFFFF0000u);
      a0[2 * j] = fmaf(w0, a, a0[2 * j]); a0[2 * j + 1] = fmaf(w0, b, a0[2 * j + 1]);
      a1[2 * j] = fmaf(w1, a, a1[2 * j]); a1[2 * j + 1] = fmaf(w1, b, a1[2 * j + 1]);
    }
  }
#pragma unroll
  for (int j = 0; j < 8; ++j) { red[r][0][c * 8 + j] = a0[j]; red[r][1][c * 8 + j] = a1[j]; }
  __syncthreads();
  { const int g = t >> 7, d = t & 127; float sum = 0.f;
    for (int i = 0; i < 16; ++i) sum += red[i][g][d];
    po[((size_t)(h * 2 + g) * NSPLIT + s) * DH + d] = sum;
    if (d == 0) { pml[((h * 2 + g) * NSPLIT + s) * 2] = g ? m1 : m0; pml[((h * 2 + g) * NSPLIT + s) * 2 + 1] = g ? l1 : l0; } }
}

__global__ void __launch_bounds__(576) k_comb_wo(const float* pml, const float* po, const u16* wo, float* part) {
  __shared__ float o[DH]; __shared__ float red[8][D];
  const int h = blockIdx.x, t = threadIdx.x;
  if (t < DH) {
    float M = -INFINITY;
    for (int s = 0; s < NSPLIT; ++s) M = fmaxf(M, pml[(h * NSPLIT + s) * 2]);
    float L = 0.f, acc = 0.f;
    for (int s = 0; s < NSPLIT; ++s) { float w = __expf(pml[(h * NSPLIT + s) * 2] - M); L = fmaf(w, pml[(h * NSPLIT + s) * 2 + 1], L); acc = fmaf(w, po[((size_t)h * NSPLIT + s) * DH + t], acc); }
    o[t] = rbf(acc / L);
  }
  __syncthreads();
  const int cg = t % 72, kl = t / 72;
  float acc[8] = {0, 0, 0, 0, 0, 0, 0, 0};
  for (int k = kl; k < DH; k += 8) {
    u32x4 w = *reinterpret_cast<const u32x4*>(wo + (size_t)(h * DH + k) * D + cg * 8);
    float xv = o[k];
#pragma unroll
    for (int j = 0; j < 4; ++j) { acc[2 * j] = fmaf(xv, __uint_as_float(w[j] << 16), acc[2 * j]); acc[2 * j + 1] = fmaf(xv, __uint_as_float(w[j] & 0xFFFF0000u), acc[2 * j + 1]); }
  }
#pragma unroll
  for (int j = 0; j < 8; ++j) red[kl][cg * 8 + j] = acc[j];
  __syncthreads();
  float s = 0.f;
  for (int i = 0; i < 8; ++i) s += red[i][t];
  part[h * D + t] = s;
}

__global__ void __launch_bounds__(256) k_mlp(const u16* x, const float* mod, const float* part, const u16* w1, const u16* w2, float* x1g, float* part2) {
  __shared__ float xs[D]; __shared__ float h[D]; __shared__ float red[32 * 64]; __shared__ float o[64]; __shared__ float red2[3][D];
  for (int i = threadIdx.x; i < D; i += 256) {
    float s = 0.f;
    for (int k = 0; k < HQ; ++k) s += part[k * D + i];
    float v = rbf(bf2f(x[i]) + rbf(mod[2 * D + i] * rbf(s)));
    xs[i] = v;
    if (blockIdx.x == 0) x1g[i] = v;
  }
  __syncthreads();
  ln_mod(xs, mod + 3 * D, mod + 4 * D, h, red);
  gemv_cols64(h, D, w1, F, blockIdx.x * 64, red, o);
  if (threadIdx.x < 64) { float v = rbf(o[threadIdx.x]); o[threadIdx.x] = rbf(v / (1.f + __expf(-v))); }
  __syncthreads();
  const int t = threadIdx.x;
  if (t < 216) {
    const int cg = t % 72, kl = t / 72;
    float acc[8] = {0, 0, 0, 0, 0, 0, 0, 0};
    for (int k = kl; k < 64; k += 3) {
      u32x4 w = *reinterpret_cast<const u32x4*>(w2 + (size_t)(blockIdx.x * 64 + k) * D + cg * 8);
      float xv = o[k];
#pragma unroll
      for (int j = 0; j < 4; ++j) { acc[2 * j] = fmaf(xv, __uint_as_float(w[j] << 16), acc[2 * j]); acc[2 * j + 1] = fmaf(xv, __uint_as_float(w[j] & 0xFFFF0000u), acc[2 * j + 1]); }
    }
#pragma unroll
    for (int j = 0; j < 8; ++j) red2[kl][cg * 8 + j] = acc[j];
  }
  __syncthreads();
  for (int i = t; i < D; i += 256) part2[blockIdx.x * D + i] = red2[0][i] + red2[1][i] + red2[2][i];
}

__global__ void __launch_bounds__(64) k_fin(const float* mod, const float* x1g, const float* part2, u16* out) {
  const int i = blockIdx.x * 64 + threadIdx.x;
  float s = 0.f;
  for (int k = 0; k < F / 64; ++k) s += part2[k * D + i];
  out[i] = f2bf(x1g[i] + rbf(mod[5 * D + i] * rbf(s)));
}

__device__ __forceinline__ unsigned long long rt() { return __builtin_amdgcn_s_memrealtime(); }
__global__ void __launch_bounds__(256) k_spin(const unsigned long long* st, int nwg, int meas) {
  __shared__ unsigned long long a[256][4];
  const int t = threadIdx.x;
  unsigned long long mn0 = ~0ull, mx0 = 0, mx1 = 0, mxd = 0, mnd = ~0ull, mx2 = 0;
  for (int w = 0; w < nwg; ++w) {
    unsigned long long s0 = st[w * 8], s1 = st[w * 8 + 1], s2 = st[w * 8 + 2];
    mn0 = s0 < mn0 ? s0 : mn0; mx0 = s0 > mx0 ? s0 : mx0; mx1 = s1 > mx1 ? s1 : mx1; mx2 = s2 > mx2 ? s2 : mx2;
    unsigned long long d = s2 - s0; mxd = d > mxd ? d : mxd; mnd = d < mnd ? d : mnd;
  }
  unsigned long long m;
  if (meas == 0) m = mx1 - mn0;
  else if (meas == 1) m = mx0 - mn0;
  else if (meas == 2) m = mxd;
  else if (meas == 3) m = mnd;
  else m = mx2 - mn0;
  if (m > 10000) m = 10000;
  const unsigned long long target = mn0 + 30000 + 50 * m;
  while (rt() < target) __builtin_amdgcn_s_sleep(8);
}
#define F8 "v_fma_f32 %0, %0, %1, %1\n v_fma_f32 %0, %0, %1, %1\n v_fma_f32 %0, %0, %1, %1\n v_fma_f32 %0, %0, %1, %1\n v_fma_f32 %0, %0, %1, %1\n v_fma_f32 %0, %0, %1, %1\n v_fma_f32 %0, %0, %1, %1\n v_fma_f32 %0, %0, %1, %1\n"
#define F64 F8 F8 F8 F8 F8 F8 F8 F8
#define F512 F64 F64 F64 F64 F64 F64 F64 F64
__global__ void __launch_bounds__(576) k_ic(unsigned long long* st, float* sink, const u32x4* big, const u32x4* big2) {
  const int wave = __builtin_amdgcn_readfirstlane(threadIdx.x >> 6);
  if (wave > 0) {
    if (STREAM) {
      const u32x4* pk = big + (size_t)blockIdx.x * 8192 + (threadIdx.x - 64);
      const u32x4* pv = big2 + (size_t)blockIdx.x * 8192 + (threadIdx.x - 64);
      u32x4 v[8]; u32x4 acc = {0,0,0,0};
#pragma unroll
      for (int j = 0; j < 32 + 8; ++j) {
        if (j >= 8) acc ^= v[(j - 8) % 8];
        if (j < 32) v[j % 8] = __builtin_nontemporal_load(j < 16 ? pk + j * 512 : pv + (j - 16) * 512);
        __builtin_amdgcn_sched_barrier(0);
      }
      if ((acc[0] ^ acc[1] ^ acc[2] ^ acc[3]) == 0x12345678u) sink[1] = 1.f;
    }
    return;
  }
  float x = threadIdx.x, y = 0.5f;
  if (STREAM) { unsigned long long t = rt(); while (rt() < t + 200) __builtin_amdgcn_s_sleep(1); }
  unsigned long long t0 = rt();
  asm volatile(F512 F512 : "+v"(x) : "v"(y));
  unsigned long long t1 = rt();
  if (threadIdx.x == 0) { st[blockIdx.x * 8] = t0; st[blockIdx.x * 8 + 1] = t1; st[blockIdx.x * 8 + 2] = t1; }
  if (x == 1.2345f) sink[0] = x;
}
#define PROBE_NWG 256
#define PROBE_LAUNCH k_ic<<<256, 576, 0, stream>>>((unsigned long long*)((char*)d_ws + (64 << 20)), (float*)((char*)d_ws + (128 << 20)), (const u32x4*)d_in[1], (const u32x4*)d_in[2])
extern "C" void kernel_launch(void* const* d_in, const int* in_sizes, int n_in, void* d_out, int out_size, void* d_ws, size_t ws_size, hipStream_t stream) {
  const u16* x = (const u16*)d_in[0]; const u16* K = (const u16*)d_in[1]; const u16* V = (const u16*)d_in[2];
  const u16* wq = (const u16*)d_in[3]; const u16* wo = (const u16*)d_in[4];
  const u16* w1 = (const u16*)d_in[6]; const u16* w2 = (const u16*)d_in[7];
  const u16* c = (const u16*)d_in[8]; const u16* wmod = (const u16*)d_in[9];
  float* ws = (float*)d_ws;
  float* mod = ws; float* q = ws + 4096; float* pml = ws + 8192; float* po = ws + 16384;
  float* part = po + HQ * NSPLIT * DH; float* x1g = part + HQ * D; float* part2 = x1g + 1024;
  PROBE_LAUNCH;
  k_mod<<<NMOD / 64, 256, 0, stream>>>(c, wmod, mod);
  k_q<<<HQ * DH / 64, 256, 0, stream>>>(x, mod, wq, q);
  k_attn<<<dim3(NSPLIT, HKV), 256, 0, stream>>>(q, K, V, pml, po);
  k_comb_wo<<<HQ, 576, 0, stream>>>(pml, po, wo, part);
  k_mlp<<<F / 64, 256, 0, stream>>>(x, mod, part, w1, w2, x1g, part2);
  k_fin<<<D / 64, 64, 0, stream>>>(mod, x1g, part2, (u16*)d_out);
  k_spin<<<1, 256, 0, stream>>>((const unsigned long long*)((char*)d_ws + (64 << 20)), PROBE_NWG, PROBE_MEAS);
}
```

```cpp
#define WHICH 2
#define PROBE_MEAS 3
#include <hip/hip_runtime.h>
#include <hip/hip_bf16.h>
#include <stdint.h>

#define MIMIC 1

typedef unsigned short u16;
typedef unsigned u32x4 __attribute__((ext_vector_type(4)));
constexpr int D = 576, HQ = 32, HKV = 16, SK = 8192, DH = 128, F = 4608, NMOD = 6 * D;
constexpr int NSPLIT = 32;
constexpr int KEYS = SK / NSPLIT;

__device__ __forceinline__ float bf2f(u16 b) { return __uint_as_float(((unsigned)b) << 16); }
__device__ __forceinline__ float rbf(float f) {
  unsigned u = __float_as_uint(f);
  u += 0x7FFFu + ((u >> 16) & 1u);
  return __uint_as_float(u & 0xFFFF0000u);
}
__device__ __forceinline__ u16 f2bf(float f) {
  unsigned u = __float_as_uint(f);
  u += 0x7FFFu + ((u >> 16) & 1u);
  return (u16)(u >> 16);
}
__device__ __forceinline__ float block_sum(float v, float* red) {
  for (int o = 32; o > 0; o >>= 1) v += __shfl_xor(v, o);
  __syncthreads();
  if ((threadIdx.x & 63) == 0) red[threadIdx.x >> 6] = v;
  __syncthreads();
  float s = 0.f;
  for (int i = 0; i < (int)(blockDim.x >> 6); ++i) s += red[i];
  return s;
}

__device__ __forceinline__ void gemv_cols64(const float* xs, int K, const u16* W, int N, int n0, float* red  , float* outv  ) {
  const int cg = threadIdx.x & 7, kl = threadIdx.x >> 3;
  float acc[8] = {0, 0, 0, 0, 0, 0, 0, 0};
  for (int k = kl; k < K; k += 32) {
    u32x4 w = *reinterpret_cast<const u32x4*>(W + (size_t)k * N + n0 + cg * 8);
    float xv = xs[k];
#pragma unroll
    for (int j = 0; j < 4; ++j) {
      acc[2 * j] = fmaf(xv, __uint_as_float(w[j] << 16), acc[2 * j]);
      acc[2 * j + 1] = fmaf(xv, __uint_as_float(w[j] & 0xFFFF0000u), acc[2 * j + 1]);
    }
  }
#pragma unroll
  for (int j = 0; j < 8; ++j) red[kl * 64 + cg * 8 + j] = acc[j];
  __syncthreads();
  if (threadIdx.x < 64) {
    float s = 0.f;
    for (int i = 0; i < 32; ++i) s += red[i * 64 + threadIdx.x];
    outv[threadIdx.x] = s;
  }
  __syncthreads();
}

__global__ void __launch_bounds__(256) k_mod(const u16* c, const u16* wmod, float* mod) {
  __shared__ float xs[D]; __shared__ float red[32 * 64]; __shared__ float o[64];
  for (int i = threadIdx.x; i < D; i += 256) { float v = bf2f(c[i]); xs[i] = rbf(v / (1.f + __expf(-v))); }
  __syncthreads();
  gemv_cols64(xs, D, wmod, NMOD, blockIdx.x * 64, red, o);
  if (threadIdx.x < 64) mod[blockIdx.x * 64 + threadIdx.x] = rbf(o[threadIdx.x]);
}

__device__ __forceinline__ void ln_mod(const float* xin  , const float* shift, const float* scale, float* h  , float* red) {
  float s = 0.f;
  for (int i = threadIdx.x; i < D; i += blockDim.x) s += xin[i];
  float mean = block_sum(s, red) / D;
  float v = 0.f;
  for (int i = threadIdx.x; i < D; i += blockDim.x) { float d = xin[i] - mean; v += d * d; }
  float var = rbf(block_sum(v, red) / D);
  float m = rbf(mean);
  float den = rbf(sqrtf(rbf(var + 1e-5f)));
  for (int i = threadIdx.x; i < D; i += blockDim.x) {
    float t = rbf(rbf(xin[i] - m) / den);
    h[i] = rbf(rbf(t * rbf(1.f + scale[i])) + shift[i]);
  }
  __syncthreads();
}

__global__ void __launch_bounds__(256) k_q(const u16* x, const float* mod, const u16* wq, float* q) {
  __shared__ float xs[D]; __shared__ float h[D]; __shared__ float red[32 * 64]; __shared__ float o[64];
  for (int i = threadIdx.x; i < D; i += 256) xs[i] = bf2f(x[i]);
  __syncthreads();
  ln_mod(xs, mod, mod + D, h, red);
  gemv_cols64(h, D, wq, HQ * DH, blockIdx.x * 64, red, o);
  if (threadIdx.x < 64) q[blockIdx.x * 64 + threadIdx.x] = rbf(o[threadIdx.x]);
}

__global__ void __launch_bounds__(256) k_attn(const float* q, const u16* K, const u16* V, float* pml, float* po) {
  __shared__ float sc[2][KEYS]; __shared__ float red[16][2][DH]; __shared__ float r2[8];
  const int s = blockIdx.x, h = blockIdx.y, t = threadIdx.x, c = t & 15, r = t >> 4;
  float qv[2][8];
#pragma unroll
  for (int g = 0; g < 2; ++g)
#pragma unroll
    for (int j = 0; j < 8; ++j) qv[g][j] = q[(h * 2 + g) * DH + c * 8 + j];
  const u16* kb = K + ((size_t)h * SK + s * KEYS) * DH + c * 8;
  const u16* vb = V + ((size_t)h * SK + s * KEYS) * DH + c * 8;
  u32x4 kr[KEYS / 16], vr[KEYS / 16];
#pragma unroll
  for (int i = 0; i < KEYS / 16; ++i) kr[i] = __builtin_nontemporal_load(reinterpret_cast<const u32x4*>(kb + (size_t)(r + 16 * i) * DH));
#pragma unroll
  for (int i = 0; i < KEYS / 16; ++i) vr[i] = __builtin_nontemporal_load(reinterpret_cast<const u32x4*>(vb + (size_t)(r + 16 * i) * DH));
#pragma unroll
  for (int i = 0; i < KEYS / 16; ++i) {
    float d0 = 0.f, d1 = 0.f;
#pragma unroll
    for (int j = 0; j < 4; ++j) {
      float a = __uint_as_float(kr[i][j] << 16), b = __uint_as_float(kr[i][j] & 0xFFFF0000u);
      d0 = fmaf(a, qv[0][2 * j], d0); d0 = fmaf(b, qv[0][2 * j + 1], d0);
      d1 = fmaf(a, qv[1][2 * j], d1); d1 = fmaf(b, qv[1][2 * j + 1], d1);
    }
    for (int o = 8; o > 0; o >>= 1) { d0 += __shfl_xor(d0, o); d1 += __shfl_xor(d1, o); }
    if (c == 0) { sc[0][r + 16 * i] = rbf(rbf(d0) * 0.08838834764831845f); sc[1][r + 16 * i] = rbf(rbf(d1) * 0.08838834764831845f); }
  }
  __syncthreads();
  float m0 = sc[0][t], m1 = sc[1][t];
  float s0 = m0, s1 = m1;
  for (int o = 32; o > 0; o >>= 1) { m0 = fmaxf(m0, __shfl_xor(m0, o)); m1 = fmaxf(m1, __shfl_xor(m1, o)); }
  if ((t & 63) == 0) { r2[t >> 6] = m0; r2[4 + (t >> 6)] = m1; }
  __syncthreads();
  m0 = fmaxf(fmaxf(r2[0], r2[1]), fmaxf(r2[2], r2[3]));
  m1 = fmaxf(fmaxf(r2[4], r2[5]), fmaxf(r2[6], r2[7]));
  float p0 = __expf(s0 - m0), p1 = __expf(s1 - m1);
  __syncthreads();
  sc[0][t] = p0; sc[1][t] = p1;
  for (int o = 32; o > 0; o >>= 1) { p0 += __shfl_xor(p0, o); p1 += __shfl_xor(p1, o); }
  if ((t & 63) == 0) { r2[t >> 6] = p0; r2[4 + (t >> 6)] = p1; }
  __syncthreads();
  float l0 = r2[0] + r2[1] + r2[2] + r2[3], l1 = r2[4] + r2[5] + r2[6] + r2[7];
  float a0[8] = {0, 0, 0, 0, 0, 0, 0, 0}, a1[8] = {0, 0, 0, 0, 0, 0, 0, 0};
#pragma unroll
  for (int i = 0; i < KEYS / 16; ++i) {
    float w0 = sc[0][r + 16 * i], w1 = sc[1][r + 16 * i];
#pragma unroll
    for (int j = 0; j < 4; ++j) {
      float a = __uint_as_float(vr[i][j] << 16), b = __uint_as_float(vr[i][j] & 0xFFFF0000u);
      a0[2 * j] = fmaf(w0, a, a0[2 * j]); a0[2 * j + 1] = fmaf(w0, b, a0[2 * j + 1]);
      a1[2 * j] = fmaf(w1, a, a1[2 * j]); a1[2 * j + 1] = fmaf(w1, b, a1[2 * j + 1]);
    }
  }
#pragma unroll
  for (int j = 0; j < 8; ++j) { red[r][0][c * 8 + j] = a0[j]; red[r][1][c * 8 + j] = a1[j]; }
  __syncthreads();
  { const int g = t >> 7, d = t & 127; float sum = 0.f;
    for (int i = 0; i < 16; ++i) sum += red[i][g][d];
    po[((size_t)(h * 2 + g) * NSPLIT + s) * DH + d] = sum;
    if (d == 0) { pml[((h * 2 + g) * NSPLIT + s) * 2] = g ? m1 : m0; pml[((h * 2 + g) * NSPLIT + s) * 2 + 1] = g ? l1 : l0; } }
}

__global__ void __launch_bounds__(576) k_comb_wo(const float* pml, const float* po, const u16* wo, float* part) {
  __shared__ float o[DH]; __shared__ float red[8][D];
  const int h = blockIdx.x, t = threadIdx.x;
  if (t < DH) {
    float M = -INFINITY;
    for (int s = 0; s < NSPLIT; ++s) M = fmaxf(M, pml[(h * NSPLIT + s) * 2]);
    float L = 0.f, acc = 0.f;
    for (int s = 0; s < NSPLIT; ++s) { float w = __expf(pml[(h * NSPLIT + s) * 2] - M); L = fmaf(w, pml[(h * NSPLIT + s) * 2 + 1], L); acc = fmaf(w, po[((size_t)h * NSPLIT + s) * DH + t], acc); }
    o[t] = rbf(acc / L);
  }
  __syncthreads();
  const int cg = t % 72, kl = t / 72;
  float acc[8] = {0, 0, 0, 0, 0, 0, 0, 0};
  for (int k = kl; k < DH; k += 8) {
    u32x4 w = *reinterpret_cast<const u32x4*>(wo + (size_t)(h * DH + k) * D + cg * 8);
    float xv = o[k];
#pragma unroll
    for (int j = 0; j < 4; ++j) { acc[2 * j] = fmaf(xv, __uint_as_float(w[j] << 16), acc[2 * j]); acc[2 * j + 1] = fmaf(xv, __uint_as_float(w[j] & 0xFFFF0000u), acc[2 * j + 1]); }
  }
#pragma unroll
  for (int j = 0; j < 8; ++j) red[kl][cg * 8 + j] = acc[j];
  __syncthreads();
  float s = 0.f;
  for (int i = 0; i < 8; ++i) s += red[i][t];
  part[h * D + t] = s;
}

__global__ void __launch_bounds__(256) k_mlp(const u16* x, const float* mod, const float* part, const u16* w1, const u16* w2, float* x1g, float* part2) {
  __shared__ float xs[D]; __shared__ float h[D]; __shared__ float red[32 * 64]; __shared__ float o[64]; __shared__ float red2[3][D];
  for (int i = threadIdx.x; i < D; i += 256) {
    float s = 0.f;
    for (int k = 0; k < HQ; ++k) s += part[k * D + i];
    float v = rbf(bf2f(x[i]) + rbf(mod[2 * D + i] * rbf(s)));
    xs[i] = v;
    if (blockIdx.x == 0) x1g[i] = v;
  }
  __syncthreads();
  ln_mod(xs, mod + 3 * D, mod + 4 * D, h, red);
  gemv_cols64(h, D, w1, F, blockIdx.x * 64, red, o);
  if (threadIdx.x < 64) { float v = rbf(o[threadIdx.x]); o[threadIdx.x] = rbf(v / (1.f + __expf(-v))); }
  __syncthreads();
  const int t = threadIdx.x;
  if (t < 216) {
    const int cg = t % 72, kl = t / 72;
    float acc[8] = {0, 0, 0, 0, 0, 0, 0, 0};
    for (int k = kl; k < 64; k += 3) {
      u32x4 w = *reinterpret_cast<const u32x4*>(w2 + (size_t)(blockIdx.x * 64 + k) * D + cg * 8);
      float xv = o[k];
#pragma unroll
      for (int j = 0; j < 4; ++j) { acc[2 * j] = fmaf(xv, __uint_as_float(w[j] << 16), acc[2 * j]); acc[2 * j + 1] = fmaf(xv, __uint_as_float(w[j] & 0xFFFF0000u), acc[2 * j + 1]); }
    }
#pragma unroll
    for (int j = 0; j < 8; ++j) red2[kl][cg * 8 + j] = acc[j];
  }
  __syncthreads();
  for (int i = t; i < D; i += 256) part2[blockIdx.x * D + i] = red2[0][i] + red2[1][i] + red2[2][i];
}

__global__ void __launch_bounds__(64) k_fin(const float* mod, const float* x1g, const float* part2, u16* out) {
  const int i = blockIdx.x * 64 + threadIdx.x;
  float s = 0.f;
  for (int k = 0; k < F / 64; ++k) s += part2[k * D + i];
  out[i] = f2bf(x1g[i] + rbf(mod[5 * D + i] * rbf(s)));
}

__device__ __forceinline__ unsigned long long rt() { return __builtin_amdgcn_s_memrealtime(); }
__global__ void __launch_bounds__(256) k_spin(const unsigned long long* st, int nwg, int meas) {
  __shared__ unsigned long long a[256][4];
  const int t = threadIdx.x;
  unsigned long long mn0 = ~0ull, mx0 = 0, mx1 = 0, mxd = 0, mnd = ~0ull, mx2 = 0;
  for (int w = 0; w < nwg; ++w) {
    unsigned long long s0 = st[w * 8], s1 = st[w * 8 + 1], s2 = st[w * 8 + 2];
    mn0 = s0 < mn0 ? s0 : mn0; mx0 = s0 > mx0 ? s0 : mx0; mx1 = s1 > mx1 ? s1 : mx1; mx2 = s2 > mx2 ? s2 : mx2;
    unsigned long long d = s2 - s0; mxd = d > mxd ? d : mxd; mnd = d < mnd ? d : mnd;
  }
  unsigned long long m;
  if (meas == 0) m = mx1 - mn0;
  else if (meas == 1) m = mx0 - mn0;
  else if (meas == 2) m = mxd;
  else if (meas == 3) m = mnd;
  else m = mx2 - mn0;
  if (m > 10000) m = 10000;
  const unsigned long long target = mn0 + 30000 + 50 * m;
  while (rt() < target) __builtin_amdgcn_s_sleep(8);
}
__global__ void __launch_bounds__(512) k_karg(const u32x4* big, unsigned long long* st, float* sink) {
  unsigned long long t0, t1, t2;
  asm volatile("s_memrealtime %0\n s_waitcnt lgkmcnt(0)" : "=s"(t0) :: "memory");
  asm volatile("s_memrealtime %0\n s_waitcnt lgkmcnt(0)" : "=s"(t1) : "s"(big) : "memory");
  u32x4 v = __builtin_nontemporal_load(big + (size_t)blockIdx.x * 8192 + threadIdx.x);
  asm volatile("s_waitcnt vmcnt(0)\n s_memrealtime %0\n s_waitcnt lgkmcnt(0)" : "=s"(t2) : "v"(v) : "memory");
  if (threadIdx.x == 0) { st[blockIdx.x * 8] = WHICH == 2 ? t1 : t0; st[blockIdx.x * 8 + 1] = t2; st[blockIdx.x * 8 + 2] = WHICH == 0 ? t1 : t2; }
}
#define PROBE_NWG 256
#define PROBE_LAUNCH k_karg<<<256, 512, 0, stream>>>((const u32x4*)d_in[1], (unsigned long long*)((char*)d_ws + (64 << 20)), (float*)((char*)d_ws + (128 << 20)))
extern "C" void kernel_launch(void* const* d_in, const int* in_sizes, int n_in, void* d_out, int out_size, void* d_ws, size_t ws_size, hipStream_t stream) {
  const u16* x = (const u16*)d_in[0]; const u16* K = (const u16*)d_in[1]; const u16* V = (const u16*)d_in[2];
  const u16* wq = (const u16*)d_in[3]; const u16* wo = (const u16*)d_in[4];
  const u16* w1 = (const u16*)d_in[6]; const u16* w2 = (const u16*)d_in[7];
  const u16* c = (const u16*)d_in[8]; const u16* wmod = (const u16*)d_in[9];
  float* ws = (float*)d_ws;
  float* mod = ws; float* q = ws + 4096; float* pml = ws + 8192; float* po = ws + 16384;
  float* part = po + HQ * NSPLIT * DH; float* x1g = part + HQ * D; float* part2 = x1g + 1024;
  PROBE_LAUNCH;
  k_mod<<<NMOD / 64, 256, 0, stream>>>(c, wmod, mod);
  k_q<<<HQ * DH / 64, 256, 0, stream>>>(x, mod, wq, q);
  k_attn<<<dim3(NSPLIT, HKV), 256, 0, stream>>>(q, K, V, pml, po);
  k_comb_wo<<<HQ, 576, 0, stream>>>(pml, po, wo, part);
  k_mlp<<<F / 64, 256, 0, stream>>>(x, mod, part, w1, w2, x1g, part2);
  k_fin<<<D / 64, 64, 0, stream>>>(mod, x1g, part2, (u16*)d_out);
  k_spin<<<1, 256, 0, stream>>>((const unsigned long long*)((char*)d_ws + (64 << 20)), PROBE_NWG, PROBE_MEAS);
}
```

```cpp
#include <hip/hip_runtime.h>
#include <stdint.h>

typedef unsigned u32;

struct Args {
  const void *K, *V, *wq, *wmod, *w1;
  void* ws;
  const void *x, *c, *wo, *w2;
  void* out;
  u32 tab[770];
};

static const u32 kTab[770] = {
  0x680bdb03u, 0x00043200u, 0x00000000u, 0x680bd86bu, 0x00043204u, 0x00000200u, 0x680bd8f4u, 0x00043208u, 0x00000400u,
  0x680bdbb6u, 0x0004320cu, 0x00000600u, 0x680bd912u, 0x00043210u, 0x00000800u, 0x680bde98u, 0x00043214u, 0x00000a00u,
  0x680bdd91u, 0x00043218u, 0x00000c00u, 0x680bd984u, 0x0004321cu, 0x00000e00u, 0x680bde0du, 0x00043220u, 0x00001000u,
  0x680bdc81u, 0x00043224u, 0x00001200u, 0x680bd923u, 0x00043228u, 0x00001400u, 0x680bd90eu, 0x0004322cu, 0x00001600u,
  0x680bda72u, 0x00043230u, 0x00001800u, 0x680bde96u, 0x00043234u, 0x00001a00u, 0x680bdf85u, 0x00043238u, 0x00001c00u,
  0x680bdc1du, 0x0004323cu, 0x00001e00u, 0x680bdb7eu, 0x00043201u, 0x00000080u, 0x680bdef0u, 0x00043205u, 0x00000280u,
  0x680bdf85u, 0x00043209u, 0x00000480u, 0x680bddadu, 0x0004320du, 0x00000680u, 0x680bda08u, 0x00043211u, 0x00000880u,
  0x680bdc91u, 0x00043215u, 0x00000a80u, 0x680bde0cu, 0x00043219u, 0x00000c80u, 0x680bdbbeu, 0x0004321du, 0x00000e80u,
  0x680bd800u, 0x00043221u, 0x00001080u, 0x680bd97du, 0x00043225u, 0x00001280u, 0x680bdd27u, 0x00043229u, 0x00001480u,
  0x680bd9f4u, 0x0004322du, 0x00001680u, 0x680bdcffu, 0x00043231u, 0x00001880u, 0x680bda23u, 0x00043235u, 0x00001a80u,
  0x680bde7cu, 0x00043239u, 0x00001c80u, 0x680bd879u, 0x0004323du, 0x00001e80u, 0x680bdd01u, 0x00043202u, 0x00000100u,
  0x680bd9f4u, 0x00043206u, 0x00000300u, 0x680bd96fu, 0x0004320au, 0x00000500u, 0x680bdc31u, 0x0004320eu, 0x00000700u,
  0x680bd897u, 0x00043212u, 0x00000900u, 0x680bdd0cu, 0x00043216u, 0x00000b00u, 0x680bda8bu, 0x0004321au, 0x00000d00u,
  0x680bde1au, 0x0004321eu, 0x00000f00u, 0x680bda9cu, 0x00043222u, 0x00001100u, 0x680bdc06u, 0x00043226u, 0x00001300u,
  0x680bdb22u, 0x0004322au, 0x00001500u, 0x680bde91u, 0x0004322eu, 0x00001700u, 0x680bdf85u, 0x00043232u, 0x00001900u,
  0x680bd800u, 0x00043236u, 0x00001b00u, 0x680bdcf0u, 0x0004323au, 0x00001d00u, 0x680bdf85u, 0x0004323eu, 0x00001f00u,
  0x680b9206u, 0x00043a83u, 0x00000180u, 0x680bdcf0u, 0x00043207u, 0x00000380u, 0x680b950au, 0x00043a8bu, 0x00000580u,
  0x680bdb3bu, 0x0004320fu, 0x00000780u, 0x680b93f1u, 0x00043a93u, 0x00000980u, 0x680bdf13u, 0x00043217u, 0x00000b80u,
  0x680b9111u, 0x00043a9bu, 0x00000d80u, 0x680bdd9fu, 0x0004321fu, 0x00000f80u, 0x680b959bu, 0x00043aa3u, 0x00001180u,
  0x680bde76u, 0x00043227u, 0x00001380u, 0x680b95a2u, 0x00043aabu, 0x00001580u, 0x680bdf85u, 0x0004322fu, 0x00001780u,
  0x680b92edu, 0x00043ab3u, 0x00001980u, 0x680bdd2cu, 0x00043237u, 0x00001b80u, 0x680b9000u, 0x00043abbu, 0x00001d80u,
  0x680bde95u, 0x0004323fu, 0x00001f80u, 0x000c767fu, 0x00031e80u, 0x00301180u, 0x300b5800u, 0x000341a9u, 0x00300380u,
  0x300b9375u, 0x00033b2eu, 0x00180400u, 0x300b978eu, 0x00033b2au, 0x00301700u, 0x300b94fau, 0x00033b34u, 0x00180900u,
  0x300b978eu, 0x00033b2cu, 0x00180b80u, 0x300b94a3u, 0x00033b28u, 0x00300d00u, 0x300b5882u, 0x000341adu, 0x00180e80u,
  0x300b9529u, 0x00033b30u, 0x00300080u, 0x300b59f8u, 0x000341b3u, 0x00301380u, 0x300bbe95u, 0x000035b5u, 0x00000000u,
  0x300b5989u, 0x000341b1u, 0x00180780u, 0x300b957au, 0x00033b32u, 0x00301800u, 0x300b5cc1u, 0x000341afu, 0x00180b00u,
  0x000caadau, 0x80031800u, 0x00288880u, 0x300b5da9u, 0x000341abu, 0x00301e00u, 0x000c75f1u, 0x00031e80u, 0x00300180u,
  0x000caf6bu, 0x80031800u, 0x00288480u, 0x000cabe7u, 0x80031800u, 0x00288580u, 0x000cad18u, 0x80031800u, 0x00144280u,
  0x000c7772u, 0x00031e80u, 0x00301980u, 0x000cbbfau, 0x80031600u, 0x00144300u, 0x000cb87au, 0x00031600u, 0x00181d00u,
  0x000cab29u, 0x80031800u, 0x00144780u, 0x000cb87bu, 0x80031600u, 0x00288000u, 0x000caa63u, 0x80031800u, 0x00288080u,
  0x000cbb9du, 0x00031600u, 0x00301400u, 0x000ca879u, 0x80031800u, 0x00288280u, 0x000cbeeeu, 0x00031600u, 0x00181880u,
  0x000cba9eu, 0x00031600u, 0x00180a00u, 0x000c71d7u, 0x00031e80u, 0x00180d80u, 0x000ca18au, 0x80031800u, 0x00288780u,
  0x000c7000u, 0x00031e80u, 0x00180180u, 0x000ca8e6u, 0x80031800u, 0x00144180u, 0x000c747cu, 0x00031e80u, 0x00300580u,
  0x000cbaa4u, 0x00031600u, 0x00301780u, 0x000c76e4u, 0x00031e80u, 0x00180980u, 0x000ca800u, 0x80031800u, 0x00288680u,
  0x000c7415u, 0x00031e80u, 0x00300d80u, 0x000cb8edu, 0x00031600u, 0x00300f80u, 0x000cb912u, 0x00031600u, 0x00181080u,
  0x000caf6bu, 0x80031800u, 0x00288180u, 0x000cba8bu, 0x00031600u, 0x00181400u, 0x000cbaeau, 0x80031600u, 0x00144500u,
  0x000c735fu, 0x00031e80u, 0x00300980u, 0x000cbbafu, 0x80031600u, 0x00288300u, 0x000cbd6bu, 0x00031600u, 0x00301d00u,
  0x000cb0f4u, 0x00031600u, 0x00181e80u, 0x000c5a78u, 0x80062200u, 0x00144800u, 0x000cbde0u, 0x00031600u, 0x00180200u,
  0x000c72e7u, 0x00031e80u, 0x00301580u, 0x000cba0du, 0x80031600u, 0x00288500u, 0x000cbc63u, 0x00031600u, 0x00301900u,
  0x000cbd87u, 0x00031600u, 0x00181b00u, 0x000c7183u, 0x00031e80u, 0x00301d80u, 0x000ca295u, 0x80031800u, 0x00288380u,
  0x000cbc92u, 0x00031600u, 0x00301080u, 0x000cab71u, 0x80031800u, 0x00144480u, 0x000c7095u, 0x00031e80u, 0x00181580u,
  0x000cbb81u, 0x00031600u, 0x00181780u, 0x000c7084u, 0x00031e80u, 0x00181980u, 0x000cb87bu, 0x00031600u, 0x00301b00u,
  0x000c76f7u, 0x00031e80u, 0x00181d80u, 0x000cb498u, 0x00031600u, 0x00301e80u, 0x000c7473u, 0x00031e80u, 0x00181180u,
  0x000ca36au, 0x80031800u, 0x00144080u, 0x000c767du, 0x00031e80u, 0x00180580u, 0x000cb800u, 0x00031600u, 0x00300600u,
  0x000cb800u, 0x00031600u, 0x00181900u, 0x000ca99bu, 0x80031800u, 0x00144680u, 0x000cab80u, 0x80031800u, 0x00144880u,
  0x000cb1ffu, 0x00031600u, 0x00180f80u, 0x000cbb17u, 0x80031600u, 0x00144000u, 0x000c5cfcu, 0x00062200u, 0x00181280u,
  0x000ca800u, 0x80031800u, 0x00144580u, 0x000cbdfau, 0x00031600u, 0x00180600u, 0x000cbc68u, 0x00031600u, 0x00301880u,
  0x000cb98cu, 0x00031600u, 0x00300a00u, 0x000c58ecu, 0x00062200u, 0x00180c80u, 0x000ca389u, 0x80031800u, 0x00144380u,
  0x1813d18cu, 0x00033100u, 0x00180080u, 0x1813ca6fu, 0x00033109u, 0x00181200u, 0x1813d1eau, 0x00033112u, 0x00301500u,
  0x181364acu, 0x00003f1bu, 0x00000000u, 0x1813d377u, 0x00033124u, 0x00300800u, 0x1813d61eu, 0x0003312du, 0x00301b80u,
  0x1813d786u, 0x80033136u, 0x00144200u, 0x1813ce95u, 0x0003313fu, 0x00181f00u, 0x1813d222u, 0x80033101u, 0x00288400u,
  0x1813d587u, 0x0003310au, 0x00181300u, 0x1813619eu, 0x00003f13u, 0x00000000u, 0x1813cd81u, 0x0003311cu, 0x00301600u,
  0x1813d674u, 0x00033125u, 0x00181800u, 0x1813d786u, 0x0003312eu, 0x00300b80u, 0x1813d072u, 0x00033137u, 0x00300c00u,
  0x1813cb10u, 0x00033140u, 0x00300e00u, 0x1813d08eu, 0x80033102u, 0x00144400u, 0x1813ce77u, 0x0003312fu, 0x00180280u,
  0x1813d000u, 0x00033114u, 0x00181500u, 0x1813ce93u, 0x0003311du, 0x00180700u, 0x1813d56cu, 0x00033126u, 0x00300900u,
  0x181362a9u, 0x00003f0bu, 0x00000000u, 0x1813d306u, 0x80033138u, 0x00288200u, 0x1813cc39u, 0x00033141u, 0x00181e00u,
  0x181303aeu, 0x00034b03u, 0x00300000u, 0x1813d6f1u, 0x0003310cu, 0x00300200u, 0x1813d4adu, 0x00033115u, 0x00180500u,
  0x1813cc18u, 0x0003311eu, 0x00181600u, 0x1813d112u, 0x80033127u, 0x00144100u, 0x1813d5a7u, 0x00033130u, 0x00181a80u,
  0x1813d3f2u, 0x80033139u, 0x00144700u, 0x1813c800u, 0x00033142u, 0x00301f80u, 0x1813d3f9u, 0x00033104u, 0x00180100u,
  0x1813c97bu, 0x0003310du, 0x00300280u, 0x1813d07au, 0x00033116u, 0x00181480u, 0x1813c897u, 0x0003311fu, 0x00300680u,
  0x1813d66au, 0x80033128u, 0x00144600u, 0x1813d315u, 0x00033131u, 0x00181b80u, 0x1813d000u, 0x0003313au, 0x00180d00u,
  0x18135cb2u, 0x00003f43u, 0x00000000u, 0x1813c9a9u, 0x00033105u, 0x00181000u, 0x1813caf8u, 0x0003310eu, 0x00300300u,
  0x1813cf0cu, 0x00033117u, 0x00300480u, 0x1813cf0cu, 0x00033120u, 0x00301680u, 0x1813d18cu, 0x00033129u, 0x00180880u,
  0x1813d335u, 0x00033132u, 0x00181a00u, 0x18130177u, 0x00034b3bu, 0x00301c80u, 0x1813ca1eu, 0x00033144u, 0x00180f00u,
  0x1813d70du, 0x00033106u, 0x00301100u, 0x1813cbfeu, 0x0003310fu, 0x00180300u, 0x1813d70bu, 0x00033118u, 0x00301480u,
  0x1813c910u, 0x00033121u, 0x00180680u, 0x1813d2fdu, 0x0003312au, 0x00300880u, 0x18135b8fu, 0x00003f33u, 0x00000000u,
  0x1813d211u, 0x0003313cu, 0x00180c00u, 0x1813cf87u, 0x00033145u, 0x00300f00u, 0x1813cf87u, 0x00033107u, 0x00301000u,
  0x1813c800u, 0x00033110u, 0x00301300u, 0x1813cc34u, 0x00033119u, 0x00300400u, 0x1813c800u, 0x00033122u, 0x00181700u,
  0x18136206u, 0x00003f2bu, 0x00000000u, 0x1813d112u, 0x00033134u, 0x00180a80u, 0x1813d602u, 0x0003313du, 0x00181c00u,
  0x1813ca97u, 0x00033146u, 0x00181f80u, 0x1813cf87u, 0x00033108u, 0x00300100u, 0x1813cc77u, 0x00033111u, 0x00301200u,
  0x1814357cu, 0x0000251au, 0x00000000u, 0x18135e28u, 0x00003f23u, 0x00000000u, 0x1813d283u, 0x8003312cu, 0x00288100u,
  0x1813ca30u, 0x00033135u, 0x00301a00u, 0x1813d687u, 0x0003313eu, 0x00301c00u, 0x1813cf0eu, 0x00033147u, 0x00180e00u,
  0xf0042688u, 0x80042800u, 0x01000000u, 0xf0241079u, 0x84842a01u, 0x0a000080u, 0xb0440e14u, 0x09002b99u, 0x13000000u,
  0xb063dd06u, 0x0d833120u, 0x1c181680u, 0xb08445ecu, 0x12002526u, 0x25000000u, 0xb0a3dc46u, 0x16833112u, 0x2e300a80u,
  0xf0c41b6fu, 0x9b042a11u, 0x37000880u, 0xb0e3dd2eu, 0x1f83311cu, 0x40301f00u, 0xf1242108u, 0x81042808u, 0x03000400u,
  0xf14412e8u, 0x97842a09u, 0x0c000480u, 0xf1641a64u, 0x8a042a0bu, 0x15000580u, 0xf1842189u, 0x8e84280au, 0x1e000500u,
  0xf1a425e6u, 0x9304280cu, 0x27000600u, 0xf1c41119u, 0x85842a0du, 0x30000680u, 0xf1e42f01u, 0x9c04280eu, 0x39000700u,
  0xf2041000u, 0xa0842a0fu, 0x42000780u, 0xf244240eu, 0x82042810u, 0x05000800u, 0xf26410fbu, 0x86842a03u, 0x0e000180u,
  0xb2840a0au, 0x0b002b9du, 0x17000000u, 0xb2a3da6fu, 0x0f833122u, 0x20300700u, 0xb2c3dbedu, 0x14033114u, 0x29180800u,
  0xb2e3ae21u, 0x18833795u, 0x32300b00u, 0xb303aa65u, 0x1d033793u, 0x3b181c80u, 0xb323af10u, 0x21833797u, 0x44300e80u,
  0xb363ad7cu, 0x030337a3u, 0x07180000u, 0xb383ad6bu, 0x0783379bu, 0x10181380u, 0xb3a3de02u, 0x0c03311au, 0x19300500u,
  0xf3c4170cu, 0x90842a05u, 0x22000280u, 0xb3e3d98du, 0x95033124u, 0x2b288600u, 0xf4042095u, 0x99842806u, 0x34000300u,
  0xb423e515u, 0x9e03311eu, 0x3d288700u, 0xf444151du, 0xa2842a07u, 0x46000380u, 0xb103df0cu, 0x04033118u, 0x7f181100u,
  0x3003ae01u, 0x088337a1u, 0x7f180380u, 0xb223df85u, 0x0d033116u, 0x7f180480u, 0x3003ac91u, 0x1183379fu, 0x7f300780u,
  0xf3442000u, 0x96042802u, 0x7f000100u, 0x3003af11u, 0x1a8337a5u, 0x7f301a80u, 0xf464246cu, 0x9f042804u, 0x7f000200u,
  0x30040e14u, 0x23802ba7u, 0x7f000000u, 0x00000000u, 0x00000000u};

__global__ void __launch_bounds__(1024, 4) fused_block(const Args a) {
#if defined(__HIP_DEVICE_COMPILE__)
  __shared__ __attribute__((aligned(1024))) unsigned char lds[131072];
  asm volatile(
    "s_load_dwordx16 s[4:19], s[0:1], 0x0\n"
    "s_mul_i32 s3, s2, 12\n"
    "s_add_u32 s54, s0, s3\n"
    "s_addc_u32 s55, s1, 0\n"
    "s_load_dwordx4 s[28:31], s[54:55], 0x58\n"
    "s_load_dwordx4 s[20:23], s[0:1], 0x40\n"
    "s_load_dwordx2 s[24:25], s[0:1], 0x50\n"
    "v_lshrrev_b32 v2, 6, v0\n"
    "v_lshrrev_b32 v5, 3, v0\n"
    "v_readfirstlane_b32 s26, v2\n"
    "v_and_b32 v7, 7, v0\n"
    "v_and_b32 v6, 6, v5\n"
    "v_xor_b32 v6, v6, v7\n"
    "v_lshlrev_b32 v6, 4, v6\n"
    "s_mov_b32 s47, 0x20000\n"
    "s_movk_i32 s57, 0x2000\n"
    "s_movk_i32 m0, 1344\n"
    "s_lshl_b32 s58, s26, 10\n"
    "s_addk_i32 s58, 24576\n"
    "s_setprio 3\n"
    "s_waitcnt lgkmcnt(0)\n"
    "s_memrealtime s[74:75]\n"
    "s_bfe_u32 s3, s29, 0x30010\n"
    "s_cmp_ge_u32 s26, s3\n"
    "s_cbranch_scc1 .Lk_nofirst\n"
    "s_cmp_lt_i32 s29, 0\n"
    "s_cselect_b64 s[44:45], s[10:11], s[8:9]\n"
    "s_cselect_b32 s54, 6912, s57\n"
    "s_mul_i32 s46, s54, 576\n"
    "s_and_b32 s30, s30, 0xffffff\n"
    "s_bitcmp1_b32 s28, 30\n"
    "s_cbranch_scc1 .Lk_own\n"
    "v_mul_u32_u24 v4, s54, v0\n"
    "buffer_load_dword v4, s[44:47], s30 offen lds\n"
    "s_branch .Lk_nofirst\n"
    ".Lk_own:\n"
    "s_mov_b32 m0, s58\n"
    "v_mad_u32_u24 v4, v5, s54, v6\n"
    "s_lshl_b32 s55, s54, 5\n"
    "s_mov_b32 s3, 5\n"
    ".Lkun1:\n"
    "buffer_load_dwordx4 v4, s[44:47], s30 offen nt lds\n"
    "s_add_u32 m0, m0, 0x1000\n"
    "s_add_u32 s30, s30, s55\n"
    "s_sub_u32 s3, s3, 1\n"
    "s_cmp_lg_u32 s3, 0\n"
    "s_cbranch_scc1 .Lkun1\n"
    ".Lk_nofirst:\n"
    "v_and_b32 v1, 63, v0\n"
    "v_lshlrev_b32 v91, 4, v1\n"
    "s_mov_b64 s[40:41], s[14:15]\n"
    "s_mov_b32 s42, 856064\n"
    "s_mov_b32 s43, s47\n"
    "s_movk_i32 s50, 1152\n"
    "s_mov_b32 s51, s47\n"
    "s_and_b32 s36, s26, 3\n"
    "v_lshlrev_b32 v2, 2, v2\n"
    "s_and_b32 s27, s2, 15\n"
    "s_lshr_b32 s33, s2, 4\n"
    "s_mov_b32 s38, 0x80000\n"
    "s_bitcmp1_b32 s28, 29\n"
    "s_cbranch_scc0 .Lk_init\n"
    "s_sub_u32 s3, s26, 4\n"
    "s_cmp_lt_u32 s3, 3\n"
    "s_cbranch_scc0 .Lk_init\n"
    "s_mul_i32 s3, s3, 48\n"
    "v_min_u32 v8, 47, v1\n"
    "v_add_u32 v8, s3, v8\n"
    "v_lshlrev_b32 v9, 3, v8\n"
    "s_bitcmp1_b32 s28, 27\n"
    "s_cselect_b64 s[48:49], s[16:17], s[18:19]\n"
    "buffer_load_dwordx2 v[10:11], v9, s[48:51], 0 offen\n"
    "buffer_load_dwordx4 v[16:19], v91, s[48:51], 0 offen\n"
    "buffer_load_dwordx4 v[20:23], v91, s[48:51], 0 offen offset:1024\n"
    "buffer_load_dword v121, off, s[40:43], 0 offset:0 sc1\n"
    "s_waitcnt lgkmcnt(0)\n"
    "s_barrier\n"
    "s_branch .Lk_act\n"
    ".Lk_init:\n"
    "v_lshlrev_b32 v7, 2, v0\n"
    "v_add_u32 v7, -3072, v7\n"
    "v_mov_b32 v125, 0\n"
    "v_cmp_gt_u32 vcc, 768, v7\n"
    "s_mov_b64 exec, vcc\n"
    "ds_write_b32 v7, v125\n"
    "s_mov_b64 exec, -1\n"
    "s_waitcnt lgkmcnt(0)\n"
    "s_barrier\n"
    "s_cmp_gt_u32 s26, 3\n"
    "s_cbranch_scc1 .Lk_bar\n"
    "s_bitcmp1_b32 s28, 30\n"
    "s_cbranch_scc0 .Lk_bar\n"
    "s_mov_b32 s3, 13\n"
    ".Lkun2:\n"
    "buffer_load_dwordx4 v4, s[44:47], s30 offen nt lds\n"
    "s_add_u32 m0, m0, 0x1000\n"
    "s_add_u32 s30, s30, s55\n"
    "s_sub_u32 s3, s3, 1\n"
    "s_cmp_lg_u32 s3, 0\n"
    "s_cbranch_scc1 .Lkun2\n"
    "s_branch .Lk_head\n"
    ".Lk_bar:\n"
    "s_cmp_gt_u32 s26, 11\n"
    "s_cbranch_scc1 .Lk_dry\n"
    "s_cmp_gt_u32 s26, 3\n"
    "s_cbranch_scc1 .Lk_kv\n"
    "s_bfe_u32 s3, s28, 0x2001b\n"
    "s_cbranch_scc0 .Lk_kv\n"
    "s_cmp_eq_u32 s3, 2\n"
    "s_cselect_b64 s[44:45], s[10:11], s[12:13]\n"
    "s_cselect_b32 s54, 27, 36\n"
    "s_lshl_b32 s54, s54, 8\n"
    "s_mul_i32 s46, s54, 576\n"
    "s_and_b32 s30, s29, 127\n"
    "s_lshl_b32 s30, s30, 7\n"
    "s_mov_b32 m0, s58\n"
    "v_mad_u32_u24 v4, v5, s54, v6\n"
    "s_lshl_b32 s55, s54, 5\n"
    "s_mov_b32 s3, 18\n"
    "s_sleep 8\n"
    ".Lkun3:\n"
    "buffer_load_dwordx4 v4, s[44:47], s30 offen nt lds\n"
    "s_add_u32 m0, m0, 0x1000\n"
    "s_add_u32 s30, s30, s55\n"
    "s_sleep 4\n"
    "s_sub_u32 s3, s3, 1\n"
    "s_cmp_lg_u32 s3, 0\n"
    "s_cbranch_scc1 .Lkun3\n"
    "s_bitcmp1_b32 s28, 29\n"
    "s_cbranch_scc1 .Lk_head\n"
    ".Lk_kv:\n"
    "s_and_b32 s35, s29, 127\n"
    "s_bfe_u32 s37, s28, 0x20013\n"
    "s_bfe_u32 s54, s28, 0x4000f\n"
    "s_bfe_u32 s55, s28, 0x4000b\n"
    "s_and_b32 s57, s28, 0x7ff\n"
    "s_cmp_lt_u32 s26, s55\n"
    "s_addc_u32 s34, s54, 0\n"
    "s_mul_i32 s54, s54, s26\n"
    "s_min_u32 s55, s55, s26\n"
    "s_add_u32 s57, s57, s54\n"
    "s_add_u32 s57, s57, s55\n"
    "s_lshl_b32 s57, s57, 10\n"
    "s_lshl_b32 s3, s27, 21\n"
    "s_add_u32 s57, s57, s3\n"
    "s_add_u32 s48, s6, s57\n"
    "s_addc_u32 s49, s7, 0\n"
    "s_lshl_b32 s50, s34, 10\n"
    "s_mov_b32 s54, s26\n"
    "s_add_u32 s55, s26, 16\n"
    "s_bitcmp1_b32 s28, 29\n"
    "s_cbranch_scc0 .Lk_rank\n"
    "s_cmp_lt_u32 s26, 7\n"
    "s_cselect_b32 s54, 9, -7\n"
    "s_cselect_b32 s3, 0, -14\n"
    "s_add_u32 s54, s54, s26\n"
    "s_cmp_lt_u32 s26, 7\n"
    "s_cselect_b32 s3, s3, 0\n"
    "s_add_u32 s55, s55, s3\n"
    ".Lk_rank:\n"
    "s_cmp_ge_u32 s55, 24\n"
    "s_cselect_b32 s81, 1, 0\n"
    "s_bfe_u32 s3, s28, 0x8000b\n"
    "s_bfe_u32 s80, s29, 0x90007\n"
    "s_sub_u32 s80, s80, 24\n"
    "s_mul_i32 s78, s54, s3\n"
    "s_lshr_b32 s78, s78, 4\n"
    "s_add_u32 s78, s78, s80\n"
    "s_mul_i32 s79, s55, s3\n"
    "s_lshr_b32 s79, s79, 4\n"
    "s_add_u32 s79, s79, s80\n"
    "s_mov_b32 s83, s80\n"
    "s_lshl_b32 s3, s3, 1\n"
    "s_add_u32 s80, s80, s3\n"
    "v_lshlrev_b32 v88, 6, v1\n"
    "v_and_b32 v88, 0x300, v88\n"
    "v_lshlrev_b32 v4, 2, v1\n"
    "v_and_b32 v4, 0xc0, v4\n"
    "v_lshlrev_b32 v91, 4, v1\n"
    "v_and_b32 v5, 48, v91\n"
    "v_or3_b32 v88, v88, v4, v5\n"
    "v_add_u32 v89, 0x1000, v88\n"
    "v_add_u32 v90, 0x2000, v88\n"
    "v_add_u32 v92, 0x1000, v91\n"
    "v_add_u32 v93, 0x2000, v91\n"
    "s_mov_b32 s56, s54\n"
    "s_lshl_b32 s3, s54, 2\n"
    "v_mov_b32 v94, s3\n"
    "s_lshl_b32 s3, s55, 2\n"
    "v_mov_b32 v127, s3\n"
    "s_setprio 2\n"
    "s_cmp_lg_u32 s26, 4\n"
    "s_cbranch_scc1 .Lk_noclr\n"
    "s_cmp_lg_u32 s37, 2\n"
    "s_cbranch_scc1 .Lk_noclr\n"
    "s_mul_i32 s3, s35, 2560\n"
    "s_add_u32 s3, s3, 378880\n"
    "v_mov_b32 v104, -1\n"
    "v_mov_b32 v105, -1\n"
    "v_mov_b32 v106, -1\n"
    "v_mov_b32 v107, -1\n"
    "buffer_store_dwordx4 v[104:107], v91, s[40:43], s3 offen sc1\n"
    "buffer_store_dwordx4 v[104:107], v91, s[40:43], s3 offen offset:1024 sc1\n"
    "s_mov_b32 exec_hi, 0\n"
    "buffer_store_dwordx4 v[104:107], v91, s[40:43], s3 offen offset:2048 sc1\n"
    "s_mov_b64 exec, -1\n"
    ".Lk_noclr:\n"
    "s_sub_u32 s3, s26, 7\n"
    "s_cmp_lt_u32 s3, 5\n"
    "s_cbranch_scc1 .Lk_extra\n"
    ".Lk_noextra:\n"
    "s_add_u32 s44, s4, s57\n"
    "s_addc_u32 s45, s5, 0\n"
    "s_mov_b32 s46, s50\n"
    "s_mul_i32 s78, s78, 930\n"
    "s_ashr_i32 s78, s78, 8\n"
    "s_branch .Lkut5\n"
    ".Lkut4:\n"
    "s_sleep 1\n"
    ".Lkut5:\n"
    "s_memrealtime s[76:77]\n"
    "s_waitcnt lgkmcnt(0)\n"
    "s_sub_u32 s76, s76, s74\n"
    "s_cmp_lt_i32 s76, s78\n"
    "s_cbranch_scc1 .Lkut4\n"
    "s_cmp_lt_u32 s56, 1\n"
    "s_cbranch_scc1 .Lk_kgo\n"
    "s_branch .Lkfw7\n"
    ".Lkfw6:\n"
    "s_sleep 1\n"
    "s_cmp_eq_u32 s38, 0\n"
    "s_cbranch_scc1 .Lkfw8\n"
    "s_sub_u32 s38, s38, 1\n"
    ".Lkfw7:\n"
    "ds_read_b32 v95, v94 offset:636\n"
    "s_waitcnt lgkmcnt(0)\n"
    "v_cmp_eq_u32 vcc, 0, v95\n"
    "s_cbranch_vccnz .Lkfw6\n"
    ".Lkfw8:\n"
    ".Lk_kgo:\n"
    "s_cmp_lg_u32 s26, 8\n"
    "s_cbranch_scc1 .Lk_kgo2\n"
    "buffer_load_dwordx4 v[112:115], off, s[40:43], 0 offset:0 sc1\n"
    ".Lk_kgo2:\n"
    "buffer_load_dwordx4 v[8:11], v88, s[44:47], 0 offen offset:0 nt\n"
    "buffer_load_dwordx4 v[12:15], v88, s[44:47], 0 offen offset:1024 nt\n"
    "buffer_load_dwordx4 v[16:19], v88, s[44:47], 0 offen offset:2048 nt\n"
    "buffer_load_dwordx4 v[20:23], v88, s[44:47], 0 offen offset:3072 nt\n"
    "buffer_load_dwordx4 v[24:27], v89, s[44:47], 0 offen offset:0 nt\n"
    "buffer_load_dwordx4 v[28:31], v89, s[44:47], 0 offen offset:1024 nt\n"
    "buffer_load_dwordx4 v[32:35], v89, s[44:47], 0 offen offset:2048 nt\n"
    "buffer_load_dwordx4 v[36:39], v89, s[44:47], 0 offen offset:3072 nt\n"
    "buffer_load_dwordx4 v[40:43], v90, s[44:47], 0 offen offset:0 nt\n"
    "buffer_load_dwordx4 v[44:47], v90, s[44:47], 0 offen offset:1024 nt\n"
    "ds_write_b32 v94, v93 offset:640\n"
    "s_setprio 1\n"
    ".Lk_att:\n"
    "s_mov_b32 s58, 0xaaaaaaaa\n"
    "s_mov_b32 s59, 0xaaaaaaaa\n"
    "s_mov_b32 s60, 0xcccccccc\n"
    "s_mov_b32 s61, 0xcccccccc\n"
    "s_mov_b32 s62, 0xf0f0f0f0\n"
    "s_mov_b32 s63, 0xf0f0f0f0\n"
    "v_and_b32 v88, 8, v1\n"
    "v_mul_u32_u24 v88, 0x40, v88\n"
    "v_lshrrev_b32 v89, 8, v88\n"
    "v_add3_u32 v88, v88, v89, 64\n"
    "v_add_u32 v88, 0xc0, v88\n"
    "v_lshlrev_b32 v89, 16, v88\n"
    "v_or_b32 v89, 0xc0c, v89\n"
    "v_or_b32 v88, 0xc0c0000, v88\n"
    "v_cndmask_b32 v88, v88, v89, s[58:59]\n"
    "v_mov_b32 v89, 0xc0c0c0c\n"
    "s_mov_b32 s54, 0x03030303\n"
    "s_mov_b32 s55, 0x03030303\n"
    "v_cndmask_b32 v4, v89, v88, s[54:55]\n"
    "s_mov_b32 s54, 0x0c0c0c0c\n"
    "s_mov_b32 s55, 0x0c0c0c0c\n"
    "v_cndmask_b32 v5, v89, v88, s[54:55]\n"
    "s_mov_b32 s54, 0x30303030\n"
    "s_mov_b32 s55, 0x30303030\n"
    "v_cndmask_b32 v6, v89, v88, s[54:55]\n"
    "s_mov_b32 s54, 0xc0c0c0c0\n"
    "s_mov_b32 s55, 0xc0c0c0c0\n"
    "v_cndmask_b32 v7, v89, v88, s[54:55]\n"
    "v_and_b32 v88, 3, v1\n"
    "v_bfe_u32 v89, v1, 2, 1\n"
    "v_lshl_add_u32 v90, v88, 1, v89\n"
    "v_cmp_gt_u32 s[54:55], s34, v90\n"
    "v_lshl_add_u32 v90, v88, 6, v89\n"
    "v_add_u32 v90, 8, v90\n"
    "v_cmp_gt_u32 s[56:57], s34, v90\n"
    "v_and_b32 v89, 8, v1\n"
    "v_lshlrev_b32 v90, 5, v89\n"
    "v_lshl_add_u32 v90, v88, 4, v90\n"
    "v_and_b32 v88, 48, v1\n"
    "v_lshl_add_u32 v90, v88, 2, v90\n"
    "v_add_u32 v90, 832, v90\n"
    "v_mov_b32 v88, 192\n"
    "v_cndmask_b32 v120, v90, v88, s[62:63]\n"
    "v_cndmask_b32 v121, v88, v90, s[62:63]\n"
    "v_lshl_add_u32 v122, v89, 3, v2\n"
    "v_lshl_add_u32 v123, v2, 8, v91\n"
    "v_mov_b32 v124, 0xff61b1e6\n"
    "v_mov_b32 v125, 0\n"
    "s_cbranch_execz .Lk_qwait\n"
    "s_mul_i32 s79, s79, 930\n"
    "s_ashr_i32 s79, s79, 8\n"
    "s_cmp_eq_u32 s26, 8\n"
    "s_cbranch_scc1 .Lk_qpoll\n"
    "s_cmp_eq_u32 s81, 0\n"
    "s_cbranch_scc1 .Lk_vearly\n"
    "s_branch .Lkft10\n"
    ".Lkft9:\n"
    "s_sleep 1\n"
    ".Lkft10:\n"
    "ds_read_b32 v88, v125 offset:48\n"
    "s_memrealtime s[76:77]\n"
    "s_waitcnt lgkmcnt(0)\n"
    "v_cmp_eq_u32 vcc, 0, v88\n"
    "s_cbranch_vccz .Lk_kwait\n"
    "s_sub_u32 s76, s76, s74\n"
    "s_cmp_lt_i32 s76, s79\n"
    "s_cbranch_scc1 .Lkft9\n"
    "s_mov_b32 s81, 0\n"
    ".Lk_vearly:\n"
    "s_branch .Lkut12\n"
    ".Lkut11:\n"
    "s_sleep 1\n"
    ".Lkut12:\n"
    "s_memrealtime s[76:77]\n"
    "s_waitcnt lgkmcnt(0)\n"
    "s_sub_u32 s76, s76, s74\n"
    "s_cmp_lt_i32 s76, s79\n"
    "s_cbranch_scc1 .Lkut11\n"
    "s_branch .Lkfw14\n"
    ".Lkfw13:\n"
    "s_sleep 1\n"
    "s_cmp_eq_u32 s38, 0\n"
    "s_cbranch_scc1 .Lkfw15\n"
    "s_sub_u32 s38, s38, 1\n"
    ".Lkfw14:\n"
    "ds_read_b32 v88, v127 offset:636\n"
    "s_waitcnt lgkmcnt(0)\n"
    "v_cmp_eq_u32 vcc, 0, v88\n"
    "s_cbranch_vccnz .Lkfw13\n"
    ".Lkfw15:\n"
    "buffer_load_dwordx4 v[84:87], v93, s[48:51], 0 offen offset:1024 nt\n"
    "buffer_load_dwordx4 v[80:83], v93, s[48:51], 0 offen offset:0 nt\n"
    "buffer_load_dwordx4 v[76:79], v92, s[48:51], 0 offen offset:3072 nt\n"
    "buffer_load_dwordx4 v[72:75], v92, s[48:51], 0 offen offset:2048 nt\n"
    "buffer_load_dwordx4 v[68:71], v92, s[48:51], 0 offen offset:1024 nt\n"
    "buffer_load_dwordx4 v[64:67], v92, s[48:51], 0 offen offset:0 nt\n"
    "buffer_load_dwordx4 v[60:63], v91, s[48:51], 0 offen offset:3072 nt\n"
    "buffer_load_dwordx4 v[56:59], v91, s[48:51], 0 offen offset:2048 nt\n"
    "buffer_load_dwordx4 v[52:55], v91, s[48:51], 0 offen offset:1024 nt\n"
    "buffer_load_dwordx4 v[48:51], v91, s[48:51], 0 offen offset:0 nt\n"
    "ds_write_b32 v127, v124 offset:640\n"
    "ds_write_b32 v2, v124 offset:576\n"
    "s_branch .Lk_qwait\n"
    ".Lk_kwait:\n"
    "s_waitcnt vmcnt(0)\n"
    ".Lk_qwait:\n"
    "s_branch .Lkfw17\n"
    ".Lkfw16:\n"
    "s_sleep 2\n"
    "s_cmp_eq_u32 s38, 0\n"
    "s_cbranch_scc1 .Lkfw18\n"
    "s_sub_u32 s38, s38, 1\n"
    ".Lkfw17:\n"
    "ds_read_b32 v88, v125 offset:48\n"
    "s_waitcnt lgkmcnt(0)\n"
    "v_cmp_eq_u32 vcc, 0, v88\n"
    "s_cbranch_vccnz .Lkfw16\n"
    ".Lkfw18:\n"
    "ds_read_b32 v126, v125 offset:816\n"
    "ds_read_b128 v[88:91], v120\n"
    "ds_read_b128 v[92:95], v121\n"
    "s_waitcnt vmcnt(10) lgkmcnt(1)\n"
    "s_cbranch_execz .Lkrl19\n"
    "v_readfirstlane_b32 s74, v126\n"
    ".Lkrl19:\n"
    "v_mfma_f32_16x16x32_bf16 v[96:99], v[8:11], v[88:91], 0\n"
    "v_mfma_f32_16x16x32_bf16 v[100:103], v[16:19], v[88:91], 0\n"
    "v_mfma_f32_16x16x32_bf16 v[104:107], v[24:27], v[88:91], 0\n"
    "v_mfma_f32_16x16x32_bf16 v[108:111], v[32:35], v[88:91], 0\n"
    "v_mfma_f32_16x16x32_bf16 v[112:115], v[40:43], v[88:91], 0\n"
    "s_waitcnt lgkmcnt(0)\n"
    "v_mfma_f32_16x16x32_bf16 v[96:99], v[12:15], v[92:95], v[96:99]\n"
    "v_mfma_f32_16x16x32_bf16 v[100:103], v[20:23], v[92:95], v[100:103]\n"
    "v_mfma_f32_16x16x32_bf16 v[104:107], v[28:31], v[92:95], v[104:107]\n"
    "v_mfma_f32_16x16x32_bf16 v[108:111], v[36:39], v[92:95], v[108:111]\n"
    "v_mfma_f32_16x16x32_bf16 v[112:115], v[44:47], v[92:95], v[112:115]\n"
    "s_nop 3\n"
    "v_mov_b32_dpp v88, v96 quad_perm:[0,0,0,0] row_mask:0xf bank_mask:0xf\n"
    "v_add_f32_dpp v88, v97, v88 quad_perm:[1,1,1,1] row_mask:0xf bank_mask:0xf\n"
    "v_add_f32_dpp v88, v98, v88 quad_perm:[2,2,2,2] row_mask:0xf bank_mask:0xf\n"
    "v_add_f32_dpp v88, v99, v88 quad_perm:[3,3,3,3] row_mask:0xf bank_mask:0xf\n"
    "v_mov_b32_dpp v89, v100 quad_perm:[0,0,0,0] row_mask:0xf bank_mask:0xf\n"
    "v_add_f32_dpp v89, v101, v89 quad_perm:[1,1,1,1] row_mask:0xf bank_mask:0xf\n"
    "v_add_f32_dpp v89, v102, v89 quad_perm:[2,2,2,2] row_mask:0xf bank_mask:0xf\n"
    "v_add_f32_dpp v89, v103, v89 quad_perm:[3,3,3,3] row_mask:0xf bank_mask:0xf\n"
    "v_mov_b32_dpp v90, v104 quad_perm:[0,0,0,0] row_mask:0xf bank_mask:0xf\n"
    "v_add_f32_dpp v90, v105, v90 quad_perm:[1,1,1,1] row_mask:0xf bank_mask:0xf\n"
    "v_add_f32_dpp v90, v106, v90 quad_perm:[2,2,2,2] row_mask:0xf bank_mask:0xf\n"
    "v_add_f32_dpp v90, v107, v90 quad_perm:[3,3,3,3] row_mask:0xf bank_mask:0xf\n"
    "v_mov_b32_dpp v91, v108 quad_perm:[0,0,0,0] row_mask:0xf bank_mask:0xf\n"
    "v_add_f32_dpp v91, v109, v91 quad_perm:[1,1,1,1] row_mask:0xf bank_mask:0xf\n"
    "v_add_f32_dpp v91, v110, v91 quad_perm:[2,2,2,2] row_mask:0xf bank_mask:0xf\n"
    "v_add_f32_dpp v91, v111, v91 quad_perm:[3,3,3,3] row_mask:0xf bank_mask:0xf\n"
    "v_mov_b32_dpp v92, v112 quad_perm:[0,0,0,0] row_mask:0xf bank_mask:0xf\n"
    "v_add_f32_dpp v92, v113, v92 quad_perm:[1,1,1,1] row_mask:0xf bank_mask:0xf\n"
    "v_add_f32_dpp v92, v114, v92 quad_perm:[2,2,2,2] row_mask:0xf bank_mask:0xf\n"
    "v_add_f32_dpp v92, v115, v92 quad_perm:[3,3,3,3] row_mask:0xf bank_mask:0xf\n"
    "v_cndmask_b32 v88, v88, v89, s[58:59]\n"
    "v_cndmask_b32 v90, v90, v91, s[58:59]\n"
    "v_cndmask_b32 v88, v88, v90, s[60:61]\n"
    "v_cvt_pk_bf16_f32 v88, v88, v92\n"
    "v_and_b32 v89, 0xffff0000, v88\n"
    "v_lshlrev_b32 v88, 16, v88\n"
    "v_mul_f32 v88, 0x3db504f3, v88\n"
    "v_mul_f32 v89, 0x3db504f3, v89\n"
    "v_cvt_pk_bf16_f32 v88, v88, v89\n"
    "v_and_b32 v89, 0xffff0000, v88\n"
    "v_lshlrev_b32 v88, 16, v88\n"
    "v_cndmask_b32 v89, v124, v89, s[56:57]\n"
    "v_cndmask_b32 v88, v124, v88, s[54:55]\n"
    "v_max_f32 v90, v88, v89\n"
    "s_nop 1\n"
    "v_max_f32_dpp v90, v90, v90 quad_perm:[1,0,3,2] row_mask:0xf bank_mask:0xf\n"
    "s_nop 1\n"
    "v_max_f32_dpp v90, v90, v90 quad_perm:[2,3,0,1] row_mask:0xf bank_mask:0xf\n"
    "s_nop 1\n"
    "v_max_f32_dpp v90, v90, v90 row_half_mirror row_mask:0xf bank_mask:0xf\n"
    "v_mov_b32 v91, v90\n"
    "s_nop 1\n"
    "v_permlane16_swap_b32 v90, v91\n"
    "v_max_f32 v90, v90, v91\n"
    "v_mov_b32 v91, v90\n"
    "s_nop 1\n"
    "v_permlane32_swap_b32 v90, v91\n"
    "v_max_f32 v90, v90, v91\n"
    "v_sub_f32 v88, v88, v90\n"
    "v_sub_f32 v89, v89, v90\n"
    "v_mul_f32 v88, 0x3fb8aa3b, v88\n"
    "v_mul_f32 v89, 0x3fb8aa3b, v89\n"
    "v_exp_f32 v88, v88\n"
    "v_exp_f32 v89, v89\n"
    "s_nop 0\n"
    "v_add_f32 v91, v88, v89\n"
    "v_mov_b32_dpp v92, v88 row_ror:8 row_mask:0xf bank_mask:0xf\n"
    "v_mov_b32_dpp v93, v89 row_ror:8 row_mask:0xf bank_mask:0xf\n"
    "v_add_f32_dpp v91, v91, v91 quad_perm:[1,0,3,2] row_mask:0xf bank_mask:0xf\n"
    "v_cvt_pk_bf16_f32 v92, v88, v92\n"
    "v_cvt_pk_bf16_f32 v93, v89, v93\n"
    "v_add_f32_dpp v91, v91, v91 quad_perm:[2,3,0,1] row_mask:0xf bank_mask:0xf\n"
    "s_nop 0\n"
    "v_mov_b32_dpp v105, v93 row_newbcast:4 row_mask:0xf bank_mask:0xf\n"
    "v_mov_b32_dpp v104, v93 row_newbcast:0 row_mask:0xf bank_mask:0xf\n"
    "v_add_f32_dpp v91, v91, v91 row_half_mirror row_mask:0xf bank_mask:0xf\n"
    "v_mov_b32_dpp v103, v92 row_newbcast:7 row_mask:0xf bank_mask:0xf\n"
    "v_mov_b32_dpp v102, v92 row_newbcast:3 row_mask:0xf bank_mask:0xf\n"
    "v_mov_b32 v94, v91\n"
    "v_mov_b32_dpp v101, v92 row_newbcast:6 row_mask:0xf bank_mask:0xf\n"
    "v_mov_b32_dpp v100, v92 row_newbcast:2 row_mask:0xf bank_mask:0xf\n"
    "v_permlane16_swap_b32 v91, v94\n"
    "v_mov_b32_dpp v99, v92 row_newbcast:5 row_mask:0xf bank_mask:0xf\n"
    "v_mov_b32_dpp v98, v92 row_newbcast:1 row_mask:0xf bank_mask:0xf\n"
    "v_add_f32 v91, v91, v94\n"
    "v_mov_b32_dpp v97, v92 row_newbcast:4 row_mask:0xf bank_mask:0xf\n"
    "v_mov_b32_dpp v96, v92 row_newbcast:0 row_mask:0xf bank_mask:0xf\n"
    "v_mov_b32 v94, v91\n"
    "v_perm_b32 v44, 0, v105, v4\n"
    "v_perm_b32 v45, 0, v105, v5\n"
    "v_permlane32_swap_b32 v91, v94\n"
    "v_perm_b32 v46, 0, v105, v6\n"
    "v_perm_b32 v47, 0, v105, v7\n"
    "v_add_f32 v91, v91, v94\n"
    "v_perm_b32 v40, 0, v104, v4\n"
    "v_perm_b32 v41, 0, v104, v5\n"
    "ds_write2_b32 v122, v90, v91 offset0:80 offset1:112\n"
    "v_perm_b32 v42, 0, v104, v6\n"
    "v_perm_b32 v43, 0, v104, v7\n"
    "v_perm_b32 v36, 0, v103, v4\n"
    "v_perm_b32 v37, 0, v103, v5\n"
    "v_perm_b32 v38, 0, v103, v6\n"
    "v_perm_b32 v39, 0, v103, v7\n"
    "v_perm_b32 v32, 0, v102, v4\n"
    "v_perm_b32 v33, 0, v102, v5\n"
    "v_perm_b32 v34, 0, v102, v6\n"
    "v_perm_b32 v35, 0, v102, v7\n"
    "v_perm_b32 v28, 0, v101, v4\n"
    "v_perm_b32 v29, 0, v101, v5\n"
    "v_perm_b32 v30, 0, v101, v6\n"
    "v_perm_b32 v31, 0, v101, v7\n"
    "v_perm_b32 v24, 0, v100, v4\n"
    "v_perm_b32 v25, 0, v100, v5\n"
    "v_perm_b32 v26, 0, v100, v6\n"
    "v_perm_b32 v27, 0, v100, v7\n"
    "v_perm_b32 v20, 0, v99, v4\n"
    "v_perm_b32 v21, 0, v99, v5\n"
    "v_perm_b32 v22, 0, v99, v6\n"
    "v_perm_b32 v23, 0, v99, v7\n"
    "v_perm_b32 v16, 0, v98, v4\n"
    "v_perm_b32 v17, 0, v98, v5\n"
    "v_perm_b32 v18, 0, v98, v6\n"
    "v_perm_b32 v19, 0, v98, v7\n"
    "v_perm_b32 v12, 0, v97, v4\n"
    "v_perm_b32 v13, 0, v97, v5\n"
    "v_perm_b32 v14, 0, v97, v6\n"
    "v_perm_b32 v15, 0, v97, v7\n"
    "v_perm_b32 v8, 0, v96, v4\n"
    "v_perm_b32 v9, 0, v96, v5\n"
    "v_perm_b32 v10, 0, v96, v6\n"
    "v_perm_b32 v11, 0, v96, v7\n"
    "s_cbranch_execz .Lk_pv\n"
    "v_lshlrev_b32 v91, 4, v1\n"
    "v_add_u32 v92, 0x1000, v91\n"
    "v_add_u32 v93, 0x2000, v91\n"
    "s_cmp_eq_u32 s26, 15\n"
    "s_cbranch_scc1 .Lk_last\n"
    "s_cmp_eq_u32 s81, 0\n"
    "s_cbranch_scc1 .Lk_pv\n"
    "s_branch .Lkut21\n"
    ".Lkut20:\n"
    "s_sleep 1\n"
    ".Lkut21:\n"
    "s_memrealtime s[76:77]\n"
    "s_waitcnt lgkmcnt(0)\n"
    "s_sub_u32 s76, s76, s74\n"
    "s_cmp_lt_i32 s76, s79\n"
    "s_cbranch_scc1 .Lkut20\n"
    "s_branch .Lkfw23\n"
    ".Lkfw22:\n"
    "s_sleep 1\n"
    "s_cmp_eq_u32 s38, 0\n"
    "s_cbranch_scc1 .Lkfw24\n"
    "s_sub_u32 s38, s38, 1\n"
    ".Lkfw23:\n"
    "ds_read_b32 v88, v127 offset:636\n"
    "s_waitcnt lgkmcnt(0)\n"
    "v_cmp_eq_u32 vcc, 0, v88\n"
    "s_cbranch_vccnz .Lkfw22\n"
    ".Lkfw24:\n"
    "buffer_load_dwordx4 v[84:87], v93, s[48:51], 0 offen offset:1024 nt\n"
    "buffer_load_dwordx4 v[80:83], v93, s[48:51], 0 offen offset:0 nt\n"
    "buffer_load_dwordx4 v[76:79], v92, s[48:51], 0 offen offset:3072 nt\n"
    "buffer_load_dwordx4 v[72:75], v92, s[48:51], 0 offen offset:2048 nt\n"
    "buffer_load_dwordx4 v[68:71], v92, s[48:51], 0 offen offset:1024 nt\n"
    "buffer_load_dwordx4 v[64:67], v92, s[48:51], 0 offen offset:0 nt\n"
    "buffer_load_dwordx4 v[60:63], v91, s[48:51], 0 offen offset:3072 nt\n"
    "buffer_load_dwordx4 v[56:59], v91, s[48:51], 0 offen offset:2048 nt\n"
    "buffer_load_dwordx4 v[52:55], v91, s[48:51], 0 offen offset:1024 nt\n"
    "buffer_load_dwordx4 v[48:51], v91, s[48:51], 0 offen offset:0 nt\n"
    "ds_write_b32 v127, v124 offset:640\n"
    "ds_write_b32 v2, v124 offset:576\n"
    ".Lk_pv:\n"
    "s_waitcnt vmcnt(9)\n"
    "v_mfma_f32_16x16x32_bf16 v[116:119], v[44:47], v[84:87], 0\n"
    "s_waitcnt vmcnt(8)\n"
    "v_mfma_f32_16x16x32_bf16 v[116:119], v[40:43], v[80:83], v[116:119]\n"
    "s_waitcnt vmcnt(7)\n"
    "v_mfma_f32_16x16x32_bf16 v[116:119], v[36:39], v[76:79], v[116:119]\n"
    "s_waitcnt vmcnt(6)\n"
    "v_mfma_f32_16x16x32_bf16 v[116:119], v[32:35], v[72:75], v[116:119]\n"
    "s_waitcnt vmcnt(5)\n"
    "v_mfma_f32_16x16x32_bf16 v[116:119], v[28:31], v[68:71], v[116:119]\n"
    "s_waitcnt vmcnt(4)\n"
    "v_mfma_f32_16x16x32_bf16 v[116:119], v[24:27], v[64:67], v[116:119]\n"
    "s_waitcnt vmcnt(3)\n"
    "v_mfma_f32_16x16x32_bf16 v[116:119], v[20:23], v[60:63], v[116:119]\n"
    "s_waitcnt vmcnt(2)\n"
    "v_mfma_f32_16x16x32_bf16 v[116:119], v[16:19], v[56:59], v[116:119]\n"
    "s_waitcnt vmcnt(1)\n"
    "v_mfma_f32_16x16x32_bf16 v[116:119], v[12:15], v[52:55], v[116:119]\n"
    "s_waitcnt vmcnt(0)\n"
    "v_mfma_f32_16x16x32_bf16 v[116:119], v[8:11], v[48:51], v[116:119]\n"
    "s_lshr_b32 s3, s26, 2\n"
    "s_nop 6\n"
    "ds_write_b128 v123, v[116:119] offset:7744\n"
    "ds_write_b32 v2, v124 offset:128\n"
    "ds_write_b32 v127, v124 offset:640\n"
    "s_setprio 0\n"
    "s_cbranch_execz .Lk_last\n"
    "s_cmp_eq_u32 s3, 0\n"
    "s_cbranch_scc1 .Lk_gat\n"
    "s_cmp_eq_u32 s3, 1\n"
    "s_cbranch_scc1 .Lk_mid\n"
    "s_cmp_eq_u32 s3, 2\n"
    "s_cbranch_scc1 .Lk_g2\n"
    "s_branch .Lk_end\n"
    ".Lk_last:\n"
    "v_and_b32 v88, 31, v1\n"
    "v_lshlrev_b32 v88, 2, v88\n"
    "v_and_b32 v89, 15, v1\n"
    "v_and_b32 v90, 32, v1\n"
    "v_lshlrev_b32 v94, 2, v89\n"
    "v_lshl_add_u32 v95, v90, 1, v94\n"
    "s_lshl_b32 s54, s27, 4\n"
    "s_add_u32 s54, s54, s33\n"
    "s_lshl_b32 s3, s54, 8\n"
    "s_add_u32 s3, s3, 51200\n"
    "v_lshl_add_u32 v96, v90, 2, s3\n"
    "v_mov_b32 v97, 0x7ffffff0\n"
    "v_cmp_eq_u32 vcc, 0, v88\n"
    "s_nop 1\n"
    "v_cndmask_b32 v96, v97, v96, vcc\n"
    "s_lshl_b32 s54, s54, 10\n"
    "s_add_u32 s54, s54, 116736\n"
    "v_min_u32 v126, 44, v94\n"
    "v_min_u32 v121, 56, v94\n"
    "s_branch .Lkfw26\n"
    ".Lkfw25:\n"
    "s_sleep 1\n"
    "s_cmp_eq_u32 s38, 0\n"
    "s_cbranch_scc1 .Lkfw27\n"
    "s_sub_u32 s38, s38, 1\n"
    ".Lkfw26:\n"
    "ds_read_b32 v97, v88 offset:448\n"
    "s_waitcnt lgkmcnt(0)\n"
    "v_cmp_eq_u32 vcc, 0, v97\n"
    "s_cbranch_vccnz .Lkfw25\n"
    ".Lkfw27:\n"
    "ds_read_b32 v98, v95 offset:320\n"
    "ds_read_b32 v99, v95 offset:448\n"
    "s_waitcnt lgkmcnt(0)\n"
    "v_mov_b32 v100, v98\n"
    "s_nop 1\n"
    "v_max_f32_dpp v100, v100, v100 row_ror:1 row_mask:0xf bank_mask:0xf\n"
    "s_nop 1\n"
    "v_max_f32_dpp v100, v100, v100 row_ror:2 row_mask:0xf bank_mask:0xf\n"
    "s_nop 1\n"
    "v_max_f32_dpp v100, v100, v100 row_ror:4 row_mask:0xf bank_mask:0xf\n"
    "s_nop 1\n"
    "v_max_f32_dpp v100, v100, v100 row_ror:8 row_mask:0xf bank_mask:0xf\n"
    "v_sub_f32 v98, v98, v100\n"
    "v_mul_f32 v98, 0x3fb8aa3b, v98\n"
    "v_exp_f32 v120, v98\n"
    "s_nop 0\n"
    "v_mul_f32 v101, v120, v99\n"
    "s_nop 1\n"
    "v_add_f32_dpp v101, v101, v101 row_ror:1 row_mask:0xf bank_mask:0xf\n"
    "s_nop 1\n"
    "v_add_f32_dpp v101, v101, v101 row_ror:2 row_mask:0xf bank_mask:0xf\n"
    "s_nop 1\n"
    "v_add_f32_dpp v101, v101, v101 row_ror:4 row_mask:0xf bank_mask:0xf\n"
    "s_nop 1\n"
    "v_add_f32_dpp v101, v101, v101 row_ror:8 row_mask:0xf bank_mask:0xf\n"
    "v_min_u32 v100, -2, v100\n"
    "v_min_u32 v101, -2, v101\n"
    "s_cbranch_execz .Lk_lv\n"
    "buffer_store_dwordx4 v[100:103], v96, s[40:43], 0 offen sc1\n"
    "ds_write_b32 v125, v124 offset:56\n"
    "s_cmp_eq_u32 s81, 0\n"
    "s_cbranch_scc1 .Lk_lv\n"
    "s_branch .Lkut29\n"
    ".Lkut28:\n"
    "s_sleep 1\n"
    ".Lkut29:\n"
    "s_memrealtime s[76:77]\n"
    "s_waitcnt lgkmcnt(0)\n"
    "s_sub_u32 s76, s76, s74\n"
    "s_cmp_lt_i32 s76, s79\n"
    "s_cbranch_scc1 .Lkut28\n"
    "s_branch .Lkfw31\n"
    ".Lkfw30:\n"
    "s_sleep 1\n"
    "s_cmp_eq_u32 s38, 0\n"
    "s_cbranch_scc1 .Lkfw32\n"
    "s_sub_u32 s38, s38, 1\n"
    ".Lkfw31:\n"
    "ds_read_b32 v97, v127 offset:636\n"
    "s_waitcnt lgkmcnt(0)\n"
    "v_cmp_eq_u32 vcc, 0, v97\n"
    "s_cbranch_vccnz .Lkfw30\n"
    ".Lkfw32:\n"
    "buffer_load_dwordx4 v[84:87], v93, s[48:51], 0 offen offset:1024 nt\n"
    "buffer_load_dwordx4 v[80:83], v93, s[48:51], 0 offen offset:0 nt\n"
    "buffer_load_dwordx4 v[76:79], v92, s[48:51], 0 offen offset:3072 nt\n"
    "buffer_load_dwordx4 v[72:75], v92, s[48:51], 0 offen offset:2048 nt\n"
    "buffer_load_dwordx4 v[68:71], v92, s[48:51], 0 offen offset:1024 nt\n"
    "buffer_load_dwordx4 v[64:67], v92, s[48:51], 0 offen offset:0 nt\n"
    "buffer_load_dwordx4 v[60:63], v91, s[48:51], 0 offen offset:3072 nt\n"
    "buffer_load_dwordx4 v[56:59], v91, s[48:51], 0 offen offset:2048 nt\n"
    "buffer_load_dwordx4 v[52:55], v91, s[48:51], 0 offen offset:1024 nt\n"
    "buffer_load_dwordx4 v[48:51], v91, s[48:51], 0 offen offset:0 nt\n"
    "ds_write_b32 v127, v124 offset:640\n"
    "ds_write_b32 v2, v124 offset:576\n"
    ".Lk_lv:\n"
    "s_branch .Lkfw34\n"
    ".Lkfw33:\n"
    "s_sleep 1\n"
    "s_cmp_eq_u32 s38, 0\n"
    "s_cbranch_scc1 .Lkfw35\n"
    "s_sub_u32 s38, s38, 1\n"
    ".Lkfw34:\n"
    "ds_read_b32 v97, v126 offset:128\n"
    "s_waitcnt lgkmcnt(0)\n"
    "v_cmp_eq_u32 vcc, 0, v97\n"
    "s_cbranch_vccnz .Lkfw33\n"
    ".Lkfw35:\n"
    "ds_read_b128 v[96:99], v91 offset:7744\n"
    "ds_read_b128 v[100:103], v91 offset:8768\n"
    "ds_read_b128 v[104:107], v91 offset:9792\n"
    "ds_read_b128 v[108:111], v91 offset:10816\n"
    "v_mov_b32_dpp v88, v120 row_newbcast:0 row_mask:0xf bank_mask:0xf\n"
    "s_waitcnt lgkmcnt(3)\n"
    "v_pk_mul_f32 v[112:113], v[96:97], v[88:89] op_sel:[0,0] op_sel_hi:[1,0]\n"
    "v_pk_mul_f32 v[114:115], v[98:99], v[88:89] op_sel:[0,0] op_sel_hi:[1,0]\n"
    "ds_read_b128 v[96:99], v91 offset:11840\n"
    "v_mov_b32_dpp v89, v120 row_newbcast:1 row_mask:0xf bank_mask:0xf\n"
    "s_waitcnt lgkmcnt(3)\n"
    "v_pk_fma_f32 v[112:113], v[100:101], v[88:89], v[112:113] op_sel:[0,1,0] op_sel_hi:[1,1,1]\n"
    "v_pk_fma_f32 v[114:115], v[102:103], v[88:89], v[114:115] op_sel:[0,1,0] op_sel_hi:[1,1,1]\n"
    "ds_read_b128 v[100:103], v91 offset:12864\n"
    "v_mov_b32_dpp v88, v120 row_newbcast:2 row_mask:0xf bank_mask:0xf\n"
    "s_waitcnt lgkmcnt(3)\n"
    "v_pk_fma_f32 v[112:113], v[104:105], v[88:89], v[112:113] op_sel:[0,0,0] op_sel_hi:[1,0,1]\n"
    "v_pk_fma_f32 v[114:115], v[106:107], v[88:89], v[114:115] op_sel:[0,0,0] op_sel_hi:[1,0,1]\n"
    "ds_read_b128 v[104:107], v91 offset:13888\n"
    "v_mov_b32_dpp v89, v120 row_newbcast:3 row_mask:0xf bank_mask:0xf\n"
    "s_waitcnt lgkmcnt(3)\n"
    "v_pk_fma_f32 v[112:113], v[108:109], v[88:89], v[112:113] op_sel:[0,1,0] op_sel_hi:[1,1,1]\n"
    "v_pk_fma_f32 v[114:115], v[110:111], v[88:89], v[114:115] op_sel:[0,1,0] op_sel_hi:[1,1,1]\n"
    "ds_read_b128 v[108:111], v91 offset:14912\n"
    "v_mov_b32_dpp v88, v120 row_newbcast:4 row_mask:0xf bank_mask:0xf\n"
    "s_waitcnt lgkmcnt(3)\n"
    "v_pk_fma_f32 v[112:113], v[96:97], v[88:89], v[112:113] op_sel:[0,0,0] op_sel_hi:[1,0,1]\n"
    "v_pk_fma_f32 v[114:115], v[98:99], v[88:89], v[114:115] op_sel:[0,0,0] op_sel_hi:[1,0,1]\n"
    "ds_read_b128 v[96:99], v91 offset:15936\n"
    "v_mov_b32_dpp v89, v120 row_newbcast:5 row_mask:0xf bank_mask:0xf\n"
    "s_waitcnt lgkmcnt(3)\n"
    "v_pk_fma_f32 v[112:113], v[100:101], v[88:89], v[112:113] op_sel:[0,1,0] op_sel_hi:[1,1,1]\n"
    "v_pk_fma_f32 v[114:115], v[102:103], v[88:89], v[114:115] op_sel:[0,1,0] op_sel_hi:[1,1,1]\n"
    "ds_read_b128 v[100:103], v91 offset:16960\n"
    "v_mov_b32_dpp v88, v120 row_newbcast:6 row_mask:0xf bank_mask:0xf\n"
    "s_waitcnt lgkmcnt(3)\n"
    "v_pk_fma_f32 v[112:113], v[104:105], v[88:89], v[112:113] op_sel:[0,0,0] op_sel_hi:[1,0,1]\n"
    "v_pk_fma_f32 v[114:115], v[106:107], v[88:89], v[114:115] op_sel:[0,0,0] op_sel_hi:[1,0,1]\n"
    "ds_read_b128 v[104:107], v91 offset:17984\n"
    "v_mov_b32_dpp v89, v120 row_newbcast:7 row_mask:0xf bank_mask:0xf\n"
    "s_waitcnt lgkmcnt(3)\n"
    "v_pk_fma_f32 v[112:113], v[108:109], v[88:89], v[112:113] op_sel:[0,1,0] op_sel_hi:[1,1,1]\n"
    "v_pk_fma_f32 v[114:115], v[110:111], v[88:89], v[114:115] op_sel:[0,1,0] op_sel_hi:[1,1,1]\n"
    "ds_read_b128 v[108:111], v91 offset:19008\n"
    "v_mov_b32_dpp v88, v120 row_newbcast:8 row_mask:0xf bank_mask:0xf\n"
    "s_waitcnt lgkmcnt(3)\n"
    "v_pk_fma_f32 v[112:113], v[96:97], v[88:89], v[112:113] op_sel:[0,0,0] op_sel_hi:[1,0,1]\n"
    "v_pk_fma_f32 v[114:115], v[98:99], v[88:89], v[114:115] op_sel:[0,0,0] op_sel_hi:[1,0,1]\n"
    "v_mov_b32_dpp v89, v120 row_newbcast:9 row_mask:0xf bank_mask:0xf\n"
    "s_waitcnt lgkmcnt(2)\n"
    "v_pk_fma_f32 v[112:113], v[100:101], v[88:89], v[112:113] op_sel:[0,1,0] op_sel_hi:[1,1,1]\n"
    "v_pk_fma_f32 v[114:115], v[102:103], v[88:89], v[114:115] op_sel:[0,1,0] op_sel_hi:[1,1,1]\n"
    "v_mov_b32_dpp v88, v120 row_newbcast:10 row_mask:0xf bank_mask:0xf\n"
    "s_waitcnt lgkmcnt(1)\n"
    "v_pk_fma_f32 v[112:113], v[104:105], v[88:89], v[112:113] op_sel:[0,0,0] op_sel_hi:[1,0,1]\n"
    "v_pk_fma_f32 v[114:115], v[106:107], v[88:89], v[114:115] op_sel:[0,0,0] op_sel_hi:[1,0,1]\n"
    "v_mov_b32_dpp v89, v120 row_newbcast:11 row_mask:0xf bank_mask:0xf\n"
    "s_waitcnt lgkmcnt(0)\n"
    "v_pk_fma_f32 v[112:113], v[108:109], v[88:89], v[112:113] op_sel:[0,1,0] op_sel_hi:[1,1,1]\n"
    "v_pk_fma_f32 v[114:115], v[110:111], v[88:89], v[114:115] op_sel:[0,1,0] op_sel_hi:[1,1,1]\n"
    "s_branch .Lkfw37\n"
    ".Lkfw36:\n"
    "s_cmp_eq_u32 s38, 0\n"
    "s_cbranch_scc1 .Lkfw38\n"
    "s_sub_u32 s38, s38, 1\n"
    ".Lkfw37:\n"
    "ds_read_b32 v97, v121 offset:128\n"
    "s_waitcnt lgkmcnt(0)\n"
    "v_cmp_eq_u32 vcc, 0, v97\n"
    "s_cbranch_vccnz .Lkfw36\n"
    ".Lkfw38:\n"
    "ds_read_b128 v[96:99], v91 offset:20032\n"
    "ds_read_b128 v[100:103], v91 offset:21056\n"
    "ds_read_b128 v[104:107], v91 offset:22080\n"
    "v_mov_b32_dpp v88, v120 row_newbcast:12 row_mask:0xf bank_mask:0xf\n"
    "s_waitcnt lgkmcnt(2)\n"
    "v_pk_fma_f32 v[112:113], v[96:97], v[88:89], v[112:113] op_sel:[0,0,0] op_sel_hi:[1,0,1]\n"
    "v_pk_fma_f32 v[114:115], v[98:99], v[88:89], v[114:115] op_sel:[0,0,0] op_sel_hi:[1,0,1]\n"
    "v_mov_b32_dpp v89, v120 row_newbcast:13 row_mask:0xf bank_mask:0xf\n"
    "s_waitcnt lgkmcnt(1)\n"
    "v_pk_fma_f32 v[112:113], v[100:101], v[88:89], v[112:113] op_sel:[0,1,0] op_sel_hi:[1,1,1]\n"
    "v_pk_fma_f32 v[114:115], v[102:103], v[88:89], v[114:115] op_sel:[0,1,0] op_sel_hi:[1,1,1]\n"
    "v_mov_b32_dpp v88, v120 row_newbcast:14 row_mask:0xf bank_mask:0xf\n"
    "s_waitcnt lgkmcnt(0)\n"
    "v_pk_fma_f32 v[112:113], v[104:105], v[88:89], v[112:113] op_sel:[0,0,0] op_sel_hi:[1,0,1]\n"
    "v_pk_fma_f32 v[114:115], v[106:107], v[88:89], v[114:115] op_sel:[0,0,0] op_sel_hi:[1,0,1]\n"
    "v_mov_b32_dpp v88, v120 row_newbcast:15 row_mask:0xf bank_mask:0xf\n"
    "s_waitcnt vmcnt(9)\n"
    "v_mfma_f32_16x16x32_bf16 v[116:119], v[44:47], v[84:87], 0\n"
    "s_waitcnt vmcnt(8)\n"
    "v_mfma_f32_16x16x32_bf16 v[116:119], v[40:43], v[80:83], v[116:119]\n"
    "s_waitcnt vmcnt(7)\n"
    "v_mfma_f32_16x16x32_bf16 v[116:119], v[36:39], v[76:79], v[116:119]\n"
    "s_waitcnt vmcnt(6)\n"
    "v_mfma_f32_16x16x32_bf16 v[116:119], v[32:35], v[72:75], v[116:119]\n"
    "s_waitcnt vmcnt(5)\n"
    "v_mfma_f32_16x16x32_bf16 v[116:119], v[28:31], v[68:71], v[116:119]\n"
    "s_waitcnt vmcnt(4)\n"
    "v_mfma_f32_16x16x32_bf16 v[116:119], v[24:27], v[64:67], v[116:119]\n"
    "s_waitcnt vmcnt(3)\n"
    "v_mfma_f32_16x16x32_bf16 v[116:119], v[20:23], v[60:63], v[116:119]\n"
    "s_waitcnt vmcnt(2)\n"
    "v_mfma_f32_16x16x32_bf16 v[116:119], v[16:19], v[56:59], v[116:119]\n"
    "s_waitcnt vmcnt(1)\n"
    "v_mfma_f32_16x16x32_bf16 v[116:119], v[12:15], v[52:55], v[116:119]\n"
    "s_waitcnt vmcnt(0)\n"
    "v_mfma_f32_16x16x32_bf16 v[116:119], v[8:11], v[48:51], v[116:119]\n"
    "s_cbranch_execz .Lk_end\n"
    "s_nop 6\n"
    "v_pk_fma_f32 v[112:113], v[116:117], v[88:89], v[112:113] op_sel:[0,0,0] op_sel_hi:[1,0,1]\n"
    "v_pk_fma_f32 v[114:115], v[118:119], v[88:89], v[114:115] op_sel:[0,0,0] op_sel_hi:[1,0,1]\n"
    "v_min_u32 v112, -2, v112\n"
    "v_min_u32 v113, -2, v113\n"
    "v_min_u32 v114, -2, v114\n"
    "v_min_u32 v115, -2, v115\n"
    "buffer_store_dwordx4 v[112:115], v91, s[40:43], s54 offen sc1\n"
    "ds_write_b32 v125, v124 offset:60\n"
    "s_branch .Lk_end\n"
    ".Lk_g2:\n"
    "s_cmp_lg_u32 s37, 2\n"
    "s_cbranch_scc1 .Lk_watch\n"
    "v_lshrrev_b32 v41, 3, v1\n"
    "v_and_b32 v40, 6, v41\n"
    "v_and_b32 v42, 7, v1\n"
    "v_xor_b32 v40, v40, v42\n"
    "v_lshlrev_b32 v40, 4, v40\n"
    "s_mov_b64 s[44:45], s[22:23]\n"
    "s_mov_b32 s46, 5308416\n"
    "s_mul_i32 s54, s35, 73728\n"
    "v_mul_u32_u24 v41, 1152, v41\n"
    "v_add3_u32 v40, v40, v41, s54\n"
    "s_mov_b32 s59, s36\n"
    "s_mov_b32 s60, 18\n"
    "s_lshl_b32 s3, s59, 13\n"
    "s_add_u32 m0, s3, 24576\n"
    "s_lshl_b32 s61, s59, 7\n"
    "s_mov_b32 s57, 512\n"
    "s_mov_b32 s58, 0x8000\n"
    "v_bfe_u32 v80, v1, 3, 2\n"
    "v_xor_b32 v80, s36, v80\n"
    "v_lshlrev_b32 v80, 5, v80\n"
    "v_lshrrev_b32 v6, 2, v1\n"
    "v_lshl_add_u32 v6, v6, 7, v80\n"
    "v_and_b32 v80, 3, v1\n"
    "v_lshl_add_u32 v6, v80, 3, v6\n"
    "v_add_u32 v6, 24576, v6\n"
    "v_add_u32 v7, 0x10000, v6\n"
    "v_and_b32 v4, 15, v1\n"
    "v_and_b32 v5, 48, v1\n"
    "v_add_u32 v5, 24128, v5\n"
    "v_mov_b32 v8, 192\n"
    "v_cmp_eq_u32 s[54:55], 0, v4\n"
    "s_nop 1\n"
    "v_cndmask_b32 v112, v8, v5, s[54:55]\n"
    "v_cmp_eq_u32 s[54:55], 4, v4\n"
    "s_nop 1\n"
    "v_cndmask_b32 v113, v8, v5, s[54:55]\n"
    "v_cmp_eq_u32 s[54:55], 8, v4\n"
    "s_nop 1\n"
    "v_cndmask_b32 v114, v8, v5, s[54:55]\n"
    "v_cmp_eq_u32 s[54:55], 12, v4\n"
    "s_nop 1\n"
    "v_cndmask_b32 v115, v8, v5, s[54:55]\n"
    "v_cmp_eq_u32 s[54:55], 1, v4\n"
    "s_nop 1\n"
    "v_cndmask_b32 v116, v8, v5, s[54:55]\n"
    "v_cmp_eq_u32 s[54:55], 5, v4\n"
    "s_nop 1\n"
    "v_cndmask_b32 v117, v8, v5, s[54:55]\n"
    "v_cmp_eq_u32 s[54:55], 9, v4\n"
    "s_nop 1\n"
    "v_cndmask_b32 v118, v8, v5, s[54:55]\n"
    "v_cmp_eq_u32 s[54:55], 13, v4\n"
    "s_nop 1\n"
    "v_cndmask_b32 v119, v8, v5, s[54:55]\n"
    "v_cmp_eq_u32 s[54:55], 2, v4\n"
    "s_nop 1\n"
    "v_cndmask_b32 v126, v8, v5, s[54:55]\n"
    "v_lshrrev_b32 v5, 4, v1\n"
    "v_lshl_add_u32 v5, v4, 2, v5\n"
    "s_mul_i32 s3, s35, 2560\n"
    "s_mul_i32 s54, s36, 640\n"
    "s_add_u32 s3, s3, s54\n"
    "s_add_u32 s3, s3, 378880\n"
    "v_lshl_add_u32 v5, v5, 4, s3\n"
    "v_mov_b32 v8, 0x7ffffff0\n"
    "v_cmp_gt_u32 vcc, 9, v4\n"
    "s_nop 1\n"
    "v_cndmask_b32 v5, v8, v5, vcc\n"
    "v_and_b32 v41, 3, v1\n"
    "v_lshlrev_b32 v41, 2, v41\n"
    "v_lshl_add_u32 v5, v1, 2, s3\n"
    "v_cmp_gt_u32 vcc, 16, v1\n"
    "s_nop 1\n"
    "v_cndmask_b32 v4, v8, v5, vcc\n"
    "s_branch .Lkfw40\n"
    ".Lkfw39:\n"
    "s_sleep 2\n"
    "s_cmp_eq_u32 s38, 0\n"
    "s_cbranch_scc1 .Lkfw41\n"
    "s_sub_u32 s38, s38, 1\n"
    ".Lkfw40:\n"
    "ds_read_b32 v80, v41 offset:64\n"
    "s_waitcnt lgkmcnt(0)\n"
    "v_cmp_eq_u32 vcc, 0, v80\n"
    "s_cbranch_vccnz .Lkfw39\n"
    ".Lkfw41:\n"
    "s_add_u32 s80, s80, 4\n"
    ".Lk_watch:\n"
    "s_cbranch_execz .Lk_wdone\n"
    "s_cmp_lg_u32 s26, 9\n"
    "s_cbranch_scc1 .Lk_wdone\n"
    "ds_read_b32 v84, v125 offset:52\n"
    "s_add_u32 s80, s80, 21\n"
    "s_bfe_u32 s62, s2, 0x20003\n"
    "s_lshl_b32 s62, s62, 7\n"
    "s_add_u32 s62, s62, 851968\n"
    "s_mul_i32 s80, s80, 930\n"
    "s_ashr_i32 s80, s80, 8\n"
    "s_sub_u32 s80, s80, 50\n"
    "s_setprio 3\n"
    ".Lk_wl0:\n"
    "s_branch .Lkft43\n"
    ".Lkft42:\n"
    "s_sleep 1\n"
    ".Lkft43:\n"
    "ds_read_b32 v80, v125 offset:636\n"
    "s_memrealtime s[76:77]\n"
    "s_waitcnt lgkmcnt(0)\n"
    "v_cmp_eq_u32 vcc, 0, v80\n"
    "s_cbranch_vccz .Lk_wph2\n"
    "s_sub_u32 s76, s76, s74\n"
    "s_cmp_lt_i32 s76, s80\n"
    "s_cbranch_scc1 .Lkft42\n"
    "s_mov_b64 exec, 1\n"
    "buffer_store_dwordx4 v[84:87], v125, s[40:43], s62 offen sc1\n"
    "s_mov_b64 exec, -1\n"
    "s_add_u32 s62, s62, 512\n"
    "s_add_u32 s80, s80, 30\n"
    "s_cmp_lt_u32 s62, 856064\n"
    "s_cbranch_scc1 .Lk_wl0\n"
    "s_branch .Lk_wdone\n"
    ".Lk_wph2:\n"
    "s_add_u32 s80, s80, 50\n"
    ".Lk_wl1:\n"
    "s_branch .Lkft45\n"
    ".Lkft44:\n"
    "s_sleep 1\n"
    ".Lkft45:\n"
    "ds_read_b32 v80, v125 offset:60\n"
    "s_memrealtime s[76:77]\n"
    "s_waitcnt lgkmcnt(0)\n"
    "v_cmp_eq_u32 vcc, 0, v80\n"
    "s_cbranch_vccz .Lk_wdone\n"
    "s_sub_u32 s76, s76, s74\n"
    "s_cmp_lt_i32 s76, s80\n"
    "s_cbranch_scc1 .Lkft44\n"
    "s_mov_b64 exec, 1\n"
    "buffer_store_dwordx4 v[84:87], v125, s[40:43], s62 offen sc1\n"
    "s_mov_b64 exec, -1\n"
    "s_add_u32 s62, s62, 512\n"
    "s_add_u32 s80, s80, 30\n"
    "s_cmp_lt_u32 s62, 856064\n"
    "s_cbranch_scc1 .Lk_wl1\n"
    ".Lk_wdone:\n"
    "s_setprio 0\n"
    "s_cmp_lg_u32 s37, 2\n"
    "s_cbranch_scc1 .Lk_fin\n"
    "s_branch .Lkfw47\n"
    ".Lkfw46:\n"
    "s_cmp_eq_u32 s38, 0\n"
    "s_cbranch_scc1 .Lkfw48\n"
    "s_sub_u32 s38, s38, 1\n"
    ".Lkfw47:\n"
    "ds_read_b32 v80, v125 offset:60\n"
    "s_waitcnt lgkmcnt(0)\n"
    "v_cmp_eq_u32 vcc, 0, v80\n"
    "s_cbranch_vccnz .Lkfw46\n"
    ".Lkfw48:\n"
    "s_cbranch_execz .Lk_w2d\n"
    "s_sub_u32 s60, 57, s36\n"
    "s_lshr_b32 s60, s60, 2\n"
    "s_sub_u32 s62, 57, s36\n"
    "s_lshr_b32 s62, s62, 2\n"
    "s_sub_u32 s62, s60, s62\n"
    ".Lkw249:\n"
    "buffer_load_dwordx4 v40, s[44:47], s61 offen nt lds\n"
    "s_cmp_lt_u32 s59, 5\n"
    "s_cselect_b32 s3, 4, -5\n"
    "s_cselect_b32 s54, s57, 8576\n"
    "s_cselect_b32 s55, s58, -39936\n"
    "s_add_u32 m0, m0, s55\n"
    "s_add_u32 s59, s59, s3\n"
    "s_add_u32 s61, s61, s54\n"
    "s_sub_u32 s60, s60, 1\n"
    "s_cmp_lg_u32 s60, 0\n"
    "s_cbranch_scc1 .Lkw249\n"
    "ds_write_b32 v2, v124 offset:80\n"
    "s_waitcnt vmcnt(1)\n"
    "s_cmp_lg_u32 s62, 0\n"
    "s_cbranch_scc1 .Lk_w2d\n"
    "s_waitcnt vmcnt(0)\n"
    ".Lk_w2d:\n"
    "ds_write_b32 v2, v124 offset:48\n"
    "s_branch .Lkfw51\n"
    ".Lkfw50:\n"
    "s_cmp_eq_u32 s38, 0\n"
    "s_cbranch_scc1 .Lkfw52\n"
    "s_sub_u32 s38, s38, 1\n"
    ".Lkfw51:\n"
    "ds_read_b32 v80, v41 offset:80\n"
    "s_waitcnt lgkmcnt(0)\n"
    "v_cmp_eq_u32 vcc, 0, v80\n"
    "s_cbranch_vccnz .Lkfw50\n"
    ".Lkfw52:\n"
    "ds_read_b64_tr_b16 v[8:9], v6 offset:0\n"
    "ds_read_b64_tr_b16 v[10:11], v6 offset:2048\n"
    "ds_read_b64_tr_b16 v[12:13], v6 offset:4096\n"
    "ds_read_b64_tr_b16 v[16:17], v6 offset:8192\n"
    "ds_read_b64_tr_b16 v[18:19], v6 offset:10240\n"
    "ds_read_b64_tr_b16 v[20:21], v6 offset:12288\n"
    "ds_read_b64_tr_b16 v[24:25], v6 offset:16384\n"
    "ds_read_b64_tr_b16 v[26:27], v6 offset:18432\n"
    "ds_read_b64_tr_b16 v[28:29], v6 offset:20480\n"
    "ds_read_b64_tr_b16 v[32:33], v6 offset:24576\n"
    "ds_read_b64_tr_b16 v[34:35], v6 offset:26624\n"
    "ds_read_b64_tr_b16 v[36:37], v6 offset:28672\n"
    "ds_read_b64_tr_b16 v[40:41], v6 offset:32768\n"
    "ds_read_b64_tr_b16 v[42:43], v6 offset:34816\n"
    "ds_read_b64_tr_b16 v[44:45], v6 offset:36864\n"
    "ds_read_b64_tr_b16 v[48:49], v6 offset:40960\n"
    "ds_read_b64_tr_b16 v[50:51], v6 offset:43008\n"
    "ds_read_b64_tr_b16 v[52:53], v6 offset:45056\n"
    "ds_read_b64_tr_b16 v[56:57], v6 offset:49152\n"
    "ds_read_b64_tr_b16 v[58:59], v6 offset:51200\n"
    "ds_read_b64_tr_b16 v[60:61], v6 offset:53248\n"
    "ds_read_b64_tr_b16 v[64:65], v6 offset:57344\n"
    "ds_read_b64_tr_b16 v[66:67], v6 offset:59392\n"
    "ds_read_b64_tr_b16 v[68:69], v6 offset:61440\n"
    "ds_read_b64_tr_b16 v[72:73], v7 offset:0\n"
    "ds_read_b64_tr_b16 v[74:75], v7 offset:2048\n"
    "ds_read_b64_tr_b16 v[76:77], v7 offset:4096\n"
    "s_waitcnt vmcnt(0) lgkmcnt(0)\n"
    "s_cbranch_execz .Lkrl53\n"
    "s_barrier\n"
    ".Lkrl53:\n"
    "s_branch .Lkfw55\n"
    ".Lkfw54:\n"
    "s_cmp_eq_u32 s38, 0\n"
    "s_cbranch_scc1 .Lkfw56\n"
    "s_sub_u32 s38, s38, 1\n"
    ".Lkfw55:\n"
    "ds_read_b32 v80, v125 offset:32\n"
    "s_waitcnt lgkmcnt(0)\n"
    "v_cmp_eq_u32 vcc, 0, v80\n"
    "s_cbranch_vccnz .Lkfw54\n"
    ".Lkfw56:\n"
    "ds_read_b64_tr_b16 v[14:15], v6 offset:6144\n"
    "ds_read_b64_tr_b16 v[22:23], v6 offset:14336\n"
    "ds_read_b64_tr_b16 v[30:31], v6 offset:22528\n"
    "ds_read_b64_tr_b16 v[38:39], v6 offset:30720\n"
    "ds_read_b64_tr_b16 v[46:47], v6 offset:38912\n"
    "ds_read_b64_tr_b16 v[54:55], v6 offset:47104\n"
    "ds_read_b64_tr_b16 v[62:63], v6 offset:55296\n"
    "ds_read_b64_tr_b16 v[70:71], v6 offset:63488\n"
    "ds_read_b64_tr_b16 v[78:79], v7 offset:6144\n"
    "s_waitcnt lgkmcnt(0)\n"
    "s_cbranch_execz .Lkrl57\n"
    "s_barrier\n"
    ".Lkrl57:\n"
    "ds_read_b128 v[80:83], v112 offset:0\n"
    "ds_read_b128 v[84:87], v112 offset:64\n"
    "ds_read_b128 v[88:91], v113 offset:0\n"
    "ds_read_b128 v[92:95], v113 offset:64\n"
    "ds_read_b128 v[96:99], v114 offset:0\n"
    "ds_read_b128 v[100:103], v114 offset:64\n"
    "ds_read_b128 v[104:107], v115 offset:0\n"
    "ds_read_b128 v[108:111], v115 offset:64\n"
    "s_waitcnt lgkmcnt(7)\n"
    "v_mfma_f32_16x16x32_bf16 v[120:123], v[80:83], v[8:11], 0\n"
    "ds_read_b128 v[80:83], v116 offset:0\n"
    "s_waitcnt lgkmcnt(6)\n"
    "v_mfma_f32_16x16x32_bf16 v[120:123], v[84:87], v[12:15], v[120:123]\n"
    "ds_read_b128 v[84:87], v116 offset:64\n"
    "v_mfma_f32_16x16x32_bf16 v[120:123], v[88:91], v[16:19], v[120:123]\n"
    "ds_read_b128 v[88:91], v117 offset:0\n"
    "s_waitcnt lgkmcnt(6)\n"
    "v_mfma_f32_16x16x32_bf16 v[120:123], v[92:95], v[20:23], v[120:123]\n"
    "ds_read_b128 v[92:95], v117 offset:64\n"
    "v_mfma_f32_16x16x32_bf16 v[120:123], v[96:99], v[24:27], v[120:123]\n"
    "ds_read_b128 v[96:99], v118 offset:0\n"
    "s_waitcnt lgkmcnt(6)\n"
    "v_mfma_f32_16x16x32_bf16 v[120:123], v[100:103], v[28:31], v[120:123]\n"
    "ds_read_b128 v[100:103], v118 offset:64\n"
    "v_mfma_f32_16x16x32_bf16 v[120:123], v[104:107], v[32:35], v[120:123]\n"
    "ds_read_b128 v[104:107], v119 offset:0\n"
    "s_waitcnt lgkmcnt(6)\n"
    "v_mfma_f32_16x16x32_bf16 v[120:123], v[108:111], v[36:39], v[120:123]\n"
    "ds_read_b128 v[108:111], v119 offset:64\n"
    "v_mfma_f32_16x16x32_bf16 v[120:123], v[80:83], v[40:43], v[120:123]\n"
    "ds_read_b128 v[80:83], v126 offset:0\n"
    "s_waitcnt lgkmcnt(6)\n"
    "v_mfma_f32_16x16x32_bf16 v[120:123], v[84:87], v[44:47], v[120:123]\n"
    "ds_read_b128 v[84:87], v126 offset:64\n"
    "v_mfma_f32_16x16x32_bf16 v[120:123], v[88:91], v[48:51], v[120:123]\n"
    "s_waitcnt lgkmcnt(5)\n"
    "v_mfma_f32_16x16x32_bf16 v[120:123], v[92:95], v[52:55], v[120:123]\n"
    "v_mfma_f32_16x16x32_bf16 v[120:123], v[96:99], v[56:59], v[120:123]\n"
    "s_waitcnt lgkmcnt(3)\n"
    "v_mfma_f32_16x16x32_bf16 v[120:123], v[100:103], v[60:63], v[120:123]\n"
    "v_mfma_f32_16x16x32_bf16 v[120:123], v[104:107], v[64:67], v[120:123]\n"
    "s_waitcnt lgkmcnt(1)\n"
    "v_mfma_f32_16x16x32_bf16 v[120:123], v[108:111], v[68:71], v[120:123]\n"
    "v_mfma_f32_16x16x32_bf16 v[120:123], v[80:83], v[72:75], v[120:123]\n"
    "s_waitcnt lgkmcnt(0)\n"
    "v_mfma_f32_16x16x32_bf16 v[120:123], v[84:87], v[76:79], v[120:123]\n"
    "s_cbranch_execz .Lk_end\n"
    "s_nop 6\n"
    "v_min_u32 v120, -2, v120\n"
    "buffer_store_dword v120, v5, s[40:43], 0 offen sc1\n"
    "v_min_u32 v121, -2, v121\n"
    "buffer_store_dword v121, v5, s[40:43], 0 offen offset:256 sc1\n"
    "v_min_u32 v122, -2, v122\n"
    "buffer_store_dword v122, v4, s[40:43], 0 offen offset:512 sc1\n"
    "s_branch .Lk_end\n"
    ".Lk_mid:\n"
    "v_and_b32 v4, 3, v1\n"
    "v_lshlrev_b32 v5, 2, v4\n"
    "s_cmp_eq_u32 s37, 2\n"
    "s_cbranch_scc1 .Lk_g1\n"
    "s_cmp_lg_u32 s37, 1\n"
    "s_cbranch_scc1 .Lk_hw2\n"
    "s_branch .Lkfw59\n"
    ".Lkfw58:\n"
    "s_sleep 2\n"
    "s_cmp_eq_u32 s38, 0\n"
    "s_cbranch_scc1 .Lkfw60\n"
    "s_sub_u32 s38, s38, 1\n"
    ".Lkfw59:\n"
    "ds_read_b32 v7, v5 offset:480\n"
    "s_waitcnt lgkmcnt(0)\n"
    "v_cmp_eq_u32 vcc, 0, v7\n"
    "s_cbranch_vccnz .Lkfw58\n"
    ".Lkfw60:\n"
    "v_bfe_u32 v80, v1, 3, 2\n"
    "v_xor_b32 v80, s36, v80\n"
    "v_lshlrev_b32 v80, 5, v80\n"
    "v_lshrrev_b32 v6, 2, v1\n"
    "v_lshl_add_u32 v6, v6, 7, v80\n"
    "v_and_b32 v80, 3, v1\n"
    "v_lshl_add_u32 v6, v80, 3, v6\n"
    "v_add_u32 v6, 98304, v6\n"
    "v_add_u32 v7, 0x10000, v6\n"
    "v_and_b32 v4, 48, v1\n"
    "s_mul_i32 s3, s27, 1152\n"
    "s_lshl_b32 s54, s33, 7\n"
    "s_add_u32 s3, s3, s54\n"
    "s_lshl_b32 s54, s36, 5\n"
    "s_add_u32 s3, s3, s54\n"
    "s_add_u32 s3, s3, 32768\n"
    "v_lshrrev_b32 v126, 1, v4\n"
    "v_add_u32 v126, s3, v126\n"
    "v_mov_b32 v80, 0x7ffffff0\n"
    "v_cmp_eq_u32 vcc, v4, v1\n"
    "s_nop 1\n"
    "v_cndmask_b32 v126, v80, v126, vcc\n"
    "v_lshlrev_b32 v118, 4, v1\n"
    "v_add_u32 v126, s3, v118\n"
    "v_cmp_gt_u32 vcc, 8, v1\n"
    "s_nop 1\n"
    "v_cndmask_b32 v126, v80, v126, vcc\n"
    "v_and_b32 v118, 0x70, v118\n"
    "v_lshrrev_b32 v127, 1, v4\n"
    "v_add_u32 v127, s54, v127\n"
    "v_lshrrev_b32 v81, 3, v1\n"
    "v_and_b32 v80, 6, v81\n"
    "v_and_b32 v82, 7, v1\n"
    "v_xor_b32 v80, v80, v82\n"
    "v_lshlrev_b32 v80, 4, v80\n"
    "s_mov_b64 s[44:45], s[20:21]\n"
    "s_mov_b32 s46, 4718592\n"
    "s_lshl_b32 s3, s27, 2\n"
    "s_add_u32 s3, s3, s36\n"
    "s_mul_i32 s58, s3, 73728\n"
    "s_lshl_b32 s3, s33, 7\n"
    "s_add_u32 s58, s58, s3\n"
    "s_add_u32 s59, s58, 0x2000\n"
    "s_add_u32 s60, s58, 0x4000\n"
    "s_add_u32 s61, s58, 0x6000\n"
    "s_lshl_b32 s3, s36, 13\n"
    "s_add_u32 m0, s3, 98304\n"
    "v_mul_u32_u24 v81, 1152, v81\n"
    "v_add_u32 v80, v80, v81\n"
    "v_add_u32 v81, 36864, v80\n"
    "s_cbranch_execz .Lk_wol\n"
    "s_mul_i32 s3, s36, 3\n"
    "s_add_u32 s80, s80, s3\n"
    "s_add_u32 s80, s80, 0\n"
    "s_branch .Lkfw62\n"
    ".Lkfw61:\n"
    "s_sleep 1\n"
    "s_cmp_eq_u32 s38, 0\n"
    "s_cbranch_scc1 .Lkfw63\n"
    "s_sub_u32 s38, s38, 1\n"
    ".Lkfw62:\n"
    "ds_read_b32 v82, v5 offset:624\n"
    "s_waitcnt lgkmcnt(0)\n"
    "v_cmp_eq_u32 vcc, 0, v82\n"
    "s_cbranch_vccnz .Lkfw61\n"
    ".Lkfw63:\n"
    "s_mul_i32 s80, s80, 930\n"
    "s_ashr_i32 s80, s80, 8\n"
    "s_branch .Lkut65\n"
    ".Lkut64:\n"
    "s_sleep 1\n"
    ".Lkut65:\n"
    "s_memrealtime s[76:77]\n"
    "s_waitcnt lgkmcnt(0)\n"
    "s_sub_u32 s76, s76, s74\n"
    "s_cmp_lt_i32 s76, s80\n"
    "s_cbranch_scc1 .Lkut64\n"
    "s_add_u32 m0, m0, 0x1000\n"
    "s_nop 0\n"
    "buffer_load_dwordx4 v81, s[44:47], s59 offen offset:1024 nt lds\n"
    "buffer_load_dwordx4 v81, s[44:47], s60 offen offset:2048 nt lds\n"
    "buffer_load_dwordx4 v81, s[44:47], s61 offen offset:3072 nt lds\n"
    ".Lk_wol:\n"
    "ds_read_b64_tr_b16 v[8:9], v6 offset:0\n"
    "ds_read_b64_tr_b16 v[10:11], v6 offset:2048\n"
    "ds_read_b64_tr_b16 v[16:17], v6 offset:8192\n"
    "ds_read_b64_tr_b16 v[18:19], v6 offset:10240\n"
    "ds_read_b64_tr_b16 v[24:25], v6 offset:16384\n"
    "ds_read_b64_tr_b16 v[26:27], v6 offset:18432\n"
    "ds_read_b64_tr_b16 v[32:33], v6 offset:24576\n"
    "ds_read_b64_tr_b16 v[34:35], v6 offset:26624\n"
    "s_waitcnt vmcnt(0)\n"
    "ds_write_b32 v2, v124 offset:80\n"
    "s_branch .Lkfw67\n"
    ".Lkfw66:\n"
    "s_cmp_eq_u32 s38, 0\n"
    "s_cbranch_scc1 .Lkfw68\n"
    "s_sub_u32 s38, s38, 1\n"
    ".Lkfw67:\n"
    "ds_read_b32 v82, v5 offset:96\n"
    "s_waitcnt lgkmcnt(0)\n"
    "v_cmp_eq_u32 vcc, 0, v82\n"
    "s_cbranch_vccnz .Lkfw66\n"
    ".Lkfw68:\n"
    "ds_read_b64_tr_b16 v[12:13], v6 offset:4096\n"
    "ds_read_b64_tr_b16 v[14:15], v6 offset:6144\n"
    "ds_read_b64_tr_b16 v[20:21], v6 offset:12288\n"
    "ds_read_b64_tr_b16 v[22:23], v6 offset:14336\n"
    "ds_read_b64_tr_b16 v[28:29], v6 offset:20480\n"
    "ds_read_b64_tr_b16 v[30:31], v6 offset:22528\n"
    "ds_read_b64_tr_b16 v[36:37], v6 offset:28672\n"
    "ds_read_b64_tr_b16 v[38:39], v6 offset:30720\n"
    "s_waitcnt lgkmcnt(0)\n"
    "s_cbranch_execz .Lkrl69\n"
    "s_barrier\n"
    ".Lkrl69:\n"
    "ds_read_b128 v[80:83], v4 offset:2880\n"
    "ds_read_b128 v[84:87], v4 offset:2944\n"
    "ds_read_b128 v[88:91], v4 offset:3008\n"
    "ds_read_b128 v[92:95], v4 offset:3072\n"
    "ds_read_b128 v[96:99], v4 offset:3136\n"
    "ds_read_b128 v[100:103], v4 offset:3200\n"
    "ds_read_b128 v[104:107], v4 offset:3264\n"
    "ds_read_b128 v[108:111], v4 offset:3328\n"
    "s_waitcnt lgkmcnt(7)\n"
    "v_mfma_f32_16x16x32_bf16 v[120:123], v[8:11], v[80:83], 0\n"
    "s_waitcnt lgkmcnt(5)\n"
    "v_mfma_f32_16x16x32_bf16 v[120:123], v[12:15], v[84:87], v[120:123]\n"
    "v_mfma_f32_16x16x32_bf16 v[120:123], v[16:19], v[88:91], v[120:123]\n"
    "s_waitcnt lgkmcnt(3)\n"
    "v_mfma_f32_16x16x32_bf16 v[120:123], v[20:23], v[92:95], v[120:123]\n"
    "v_mfma_f32_16x16x32_bf16 v[120:123], v[24:27], v[96:99], v[120:123]\n"
    "s_waitcnt lgkmcnt(1)\n"
    "v_mfma_f32_16x16x32_bf16 v[120:123], v[28:31], v[100:103], v[120:123]\n"
    "v_mfma_f32_16x16x32_bf16 v[120:123], v[32:35], v[104:107], v[120:123]\n"
    "s_waitcnt lgkmcnt(0)\n"
    "v_mfma_f32_16x16x32_bf16 v[120:123], v[36:39], v[108:111], v[120:123]\n"
    "s_cmp_lg_u32 s36, 0\n"
    "s_nop 6\n"
    "v_cvt_pk_bf16_f32 v120, v120, v121\n"
    "v_cvt_pk_bf16_f32 v121, v122, v123\n"
    "v_min_u32 v120, -2, v120\n"
    "v_min_u32 v121, -2, v121\n"
    "ds_write_b64 v127, v[120:121] offset:24128\n"
    "s_cbranch_execz .Lk_end\n"
    "s_waitcnt lgkmcnt(0)\n"
    "s_barrier\n"
    "s_cbranch_scc1 .Lk_end\n"
    "ds_read_b128 v[120:123], v118 offset:24128\n"
    "s_waitcnt lgkmcnt(0)\n"
    "buffer_store_dwordx4 v[120:123], v126, s[40:43], 0 offen sc1\n"
    "s_branch .Lk_end\n"
    ".Lk_g1:\n"
    "s_branch .Lkfw71\n"
    ".Lkfw70:\n"
    "s_sleep 2\n"
    "s_cmp_eq_u32 s38, 0\n"
    "s_cbranch_scc1 .Lkfw72\n"
    "s_sub_u32 s38, s38, 1\n"
    ".Lkfw71:\n"
    "ds_read_b32 v6, v5 offset:448\n"
    "s_waitcnt lgkmcnt(0)\n"
    "v_cmp_eq_u32 vcc, 0, v6\n"
    "s_cbranch_vccnz .Lkfw70\n"
    ".Lkfw72:\n"
    "v_bfe_u32 v80, v1, 3, 2\n"
    "v_xor_b32 v80, s36, v80\n"
    "v_lshlrev_b32 v80, 5, v80\n"
    "v_lshrrev_b32 v6, 2, v1\n"
    "v_lshl_add_u32 v6, v6, 7, v80\n"
    "v_and_b32 v80, 3, v1\n"
    "v_lshl_add_u32 v6, v80, 3, v6\n"
    "v_add_u32 v6, 24576, v6\n"
    "v_add_u32 v7, 0x10000, v6\n"
    "ds_read_b64_tr_b16 v[8:9], v6 offset:0\n"
    "ds_read_b64_tr_b16 v[10:11], v6 offset:2048\n"
    "ds_read_b64_tr_b16 v[12:13], v6 offset:4096\n"
    "ds_read_b64_tr_b16 v[14:15], v6 offset:6144\n"
    "ds_read_b64_tr_b16 v[16:17], v6 offset:8192\n"
    "ds_read_b64_tr_b16 v[18:19], v6 offset:10240\n"
    "ds_read_b64_tr_b16 v[20:21], v6 offset:12288\n"
    "ds_read_b64_tr_b16 v[22:23], v6 offset:14336\n"
    "ds_read_b64_tr_b16 v[24:25], v6 offset:16384\n"
    "ds_read_b64_tr_b16 v[26:27], v6 offset:18432\n"
    "ds_read_b64_tr_b16 v[28:29], v6 offset:20480\n"
    "ds_read_b64_tr_b16 v[30:31], v6 offset:22528\n"
    "ds_read_b64_tr_b16 v[32:33], v6 offset:24576\n"
    "ds_read_b64_tr_b16 v[34:35], v6 offset:26624\n"
    "ds_read_b64_tr_b16 v[36:37], v6 offset:28672\n"
    "ds_read_b64_tr_b16 v[38:39], v6 offset:30720\n"
    "ds_read_b64_tr_b16 v[40:41], v6 offset:32768\n"
    "ds_read_b64_tr_b16 v[42:43], v6 offset:34816\n"
    "ds_read_b64_tr_b16 v[44:45], v6 offset:36864\n"
    "ds_read_b64_tr_b16 v[46:47], v6 offset:38912\n"
    "ds_read_b64_tr_b16 v[48:49], v6 offset:40960\n"
    "ds_read_b64_tr_b16 v[50:51], v6 offset:43008\n"
    "ds_read_b64_tr_b16 v[52:53], v6 offset:45056\n"
    "ds_read_b64_tr_b16 v[54:55], v6 offset:47104\n"
    "ds_read_b64_tr_b16 v[56:57], v6 offset:49152\n"
    "ds_read_b64_tr_b16 v[58:59], v6 offset:51200\n"
    "ds_read_b64_tr_b16 v[60:61], v6 offset:53248\n"
    "ds_read_b64_tr_b16 v[62:63], v6 offset:55296\n"
    "ds_read_b64_tr_b16 v[64:65], v6 offset:57344\n"
    "ds_read_b64_tr_b16 v[66:67], v6 offset:59392\n"
    "ds_read_b64_tr_b16 v[68:69], v6 offset:61440\n"
    "ds_read_b64_tr_b16 v[70:71], v6 offset:63488\n"
    "ds_read_b64_tr_b16 v[72:73], v7 offset:0\n"
    "ds_read_b64_tr_b16 v[74:75], v7 offset:2048\n"
    "ds_read_b64_tr_b16 v[76:77], v7 offset:4096\n"
    "ds_read_b64_tr_b16 v[78:79], v7 offset:6144\n"
    "v_mul_u32_u24 v4, 1216, v4\n"
    "v_and_b32 v80, 48, v1\n"
    "v_add_u32 v4, v4, v80\n"
    "v_and_b32 v80, 12, v1\n"
    "v_and_b32 v81, 3, v1\n"
    "v_lshlrev_b32 v81, 1, v81\n"
    "v_lshl_add_u32 v80, v80, 2, v81\n"
    "s_lshr_b32 s3, s36, 1\n"
    "s_lshl_b32 s3, s3, 6\n"
    "s_and_b32 s54, s36, 1\n"
    "s_lshl_b32 s54, s54, 3\n"
    "s_add_u32 s3, s3, s54\n"
    "v_add_u32 v126, s3, v80\n"
    "v_lshlrev_b32 v127, 1, v2\n"
    "s_waitcnt lgkmcnt(0)\n"
    "ds_write_b32 v2, v124 offset:48\n"
    "s_mov_b32 s56, 0x3ae38e39\n"
    "s_mov_b32 s57, 0x3ae38e39\n"
    "s_setprio 1\n"
    "s_cbranch_execz .Lkrl73\n"
    "s_barrier\n"
    ".Lkrl73:\n"
    "ds_read_b128 v[80:83], v4 offset:2880\n"
    "ds_read_b128 v[84:87], v4 offset:2944\n"
    "ds_read_b128 v[88:91], v4 offset:3008\n"
    "ds_read_b128 v[92:95], v4 offset:3072\n"
    "ds_read_b128 v[96:99], v4 offset:3136\n"
    "ds_read_b128 v[100:103], v4 offset:3200\n"
    "ds_read_b128 v[104:107], v4 offset:3264\n"
    "ds_read_b128 v[108:111], v4 offset:3328\n"
    "s_waitcnt lgkmcnt(7)\n"
    "v_mfma_f32_16x16x32_bf16 v[120:123], v[80:83], v[8:11], 0\n"
    "ds_read_b128 v[80:83], v4 offset:3392\n"
    "s_waitcnt lgkmcnt(6)\n"
    "v_mfma_f32_16x16x32_bf16 v[120:123], v[84:87], v[12:15], v[120:123]\n"
    "ds_read_b128 v[112:115], v125 offset:768\n"
    "ds_read_b128 v[116:119], v125 offset:784\n"
    "ds_read_b128 v[8:11], v125 offset:800\n"
    "ds_read_b128 v[84:87], v4 offset:3456\n"
    "v_mfma_f32_16x16x32_bf16 v[120:123], v[88:91], v[16:19], v[120:123]\n"
    "ds_read_b128 v[88:91], v4 offset:3520\n"
    "s_waitcnt lgkmcnt(9)\n"
    "v_mfma_f32_16x16x32_bf16 v[120:123], v[92:95], v[20:23], v[120:123]\n"
    "ds_read_b128 v[92:95], v4 offset:3584\n"
    "v_mfma_f32_16x16x32_bf16 v[120:123], v[96:99], v[24:27], v[120:123]\n"
    "ds_read_b128 v[96:99], v4 offset:3648\n"
    "s_waitcnt lgkmcnt(9)\n"
    "v_mfma_f32_16x16x32_bf16 v[120:123], v[100:103], v[28:31], v[120:123]\n"
    "ds_read_b128 v[100:103], v4 offset:3712\n"
    "v_mfma_f32_16x16x32_bf16 v[120:123], v[104:107], v[32:35], v[120:123]\n"
    "ds_read_b128 v[104:107], v4 offset:3776\n"
    "s_waitcnt lgkmcnt(9)\n"
    "v_mfma_f32_16x16x32_bf16 v[120:123], v[108:111], v[36:39], v[120:123]\n"
    "ds_read_b128 v[108:111], v4 offset:3840\n"
    "v_mfma_f32_16x16x32_bf16 v[120:123], v[80:83], v[40:43], v[120:123]\n"
    "ds_read_b128 v[80:83], v4 offset:3904\n"
    "s_waitcnt lgkmcnt(6)\n"
    "v_mfma_f32_16x16x32_bf16 v[120:123], v[84:87], v[44:47], v[120:123]\n"
    "ds_read_b128 v[84:87], v4 offset:3968\n"
    "v_pk_add_f32 v[112:113], v[112:113], v[114:115]\n"
    "v_pk_add_f32 v[116:117], v[116:117], v[118:119]\n"
    "v_mfma_f32_16x16x32_bf16 v[120:123], v[88:91], v[48:51], v[120:123]\n"
    "v_pk_add_f32 v[8:9], v[8:9], v[10:11]\n"
    "v_pk_add_f32 v[112:113], v[112:113], v[116:117]\n"
    "s_waitcnt lgkmcnt(5)\n"
    "v_mfma_f32_16x16x32_bf16 v[120:123], v[92:95], v[52:55], v[120:123]\n"
    "v_pk_add_f32 v[112:113], v[112:113], v[8:9]\n"
    "v_pk_mul_f32 v[112:113], v[112:113], s[56:57]\n"
    "v_mfma_f32_16x16x32_bf16 v[120:123], v[96:99], v[56:59], v[120:123]\n"
    "v_fma_f32 v113, -v112, v112, v113\n"
    "v_max_f32 v113, 0, v113\n"
    "s_waitcnt lgkmcnt(3)\n"
    "v_mfma_f32_16x16x32_bf16 v[120:123], v[100:103], v[60:63], v[120:123]\n"
    "v_cvt_pk_bf16_f32 v113, v113, v112\n"
    "v_and_b32 v112, 0xffff0000, v113\n"
    "v_mfma_f32_16x16x32_bf16 v[120:123], v[104:107], v[64:67], v[120:123]\n"
    "v_lshlrev_b32 v113, 16, v113\n"
    "v_add_f32 v113, 0x3727c5ac, v113\n"
    "s_waitcnt lgkmcnt(1)\n"
    "v_mfma_f32_16x16x32_bf16 v[120:123], v[108:111], v[68:71], v[120:123]\n"
    "v_cvt_pk_bf16_f32 v113, v113, v113\n"
    "v_lshlrev_b32 v113, 16, v113\n"
    "v_mfma_f32_16x16x32_bf16 v[120:123], v[80:83], v[72:75], v[120:123]\n"
    "v_sqrt_f32 v113, v113\n"
    "s_waitcnt lgkmcnt(0)\n"
    "v_mfma_f32_16x16x32_bf16 v[120:123], v[84:87], v[76:79], v[120:123]\n"
    "v_cvt_pk_bf16_f32 v113, v113, v113\n"
    "v_lshlrev_b32 v113, 16, v113\n"
    "v_rcp_f32 v113, v113\n"
    "s_nop 4\n"
    "v_add_f32 v120, v120, v121\n"
    "v_fma_f32 v120, -v112, v122, v120\n"
    "v_fma_f32 v120, v113, v120, v123\n"
    "v_cvt_pk_bf16_f32 v120, v120, v120\n"
    "v_lshlrev_b32 v120, 16, v120\n"
    "v_mul_f32 v121, 0xbfb8aa3b, v120\n"
    "v_exp_f32 v121, v121\n"
    "s_nop 0\n"
    "v_add_f32 v121, 1.0, v121\n"
    "v_rcp_f32 v121, v121\n"
    "s_nop 0\n"
    "v_mul_f32 v120, v120, v121\n"
    "v_cvt_pk_bf16_f32 v120, v120, v120\n"
    "ds_write_b16 v126, v120 offset:24128\n"
    "s_waitcnt lgkmcnt(0)\n"
    "s_cbranch_execz .Lk_end\n"
    "s_barrier\n"
    "s_branch .Lk_end\n"
    ".Lk_gat:\n"
    "v_and_b32 v5, 15, v1\n"
    "v_lshrrev_b32 v6, 4, v1\n"
    "ds_read_b32 v7, v125 offset:52\n"
    "s_waitcnt lgkmcnt(0)\n"
    "s_cmp_eq_u32 s37, 2\n"
    "s_cbranch_scc1 .Lk_st1\n"
    "s_cmp_lg_u32 s37, 1\n"
    "s_cbranch_scc1 .Lk_end\n"
    "s_lshl_b32 s58, s27, 12\n"
    "s_lshr_b32 s3, s36, 1\n"
    "s_lshl_b32 s54, s3, 7\n"
    "s_add_u32 s58, s58, s54\n"
    "s_add_u32 s58, s58, 51200\n"
    "v_add_u32 v8, v5, v6\n"
    "v_and_b32 v8, 15, v8\n"
    "v_lshl_add_u32 v8, v8, 8, s58\n"
    "s_lshl_b32 s3, s3, 8\n"
    "s_and_b32 s54, s36, 1\n"
    "s_lshl_b32 s54, s54, 4\n"
    "s_add_u32 s3, s3, s54\n"
    "v_lshl_add_u32 v10, v6, 1, s3\n"
    "v_and_b32 v11, 12, v5\n"
    "v_lshl_add_u32 v10, v11, 4, v10\n"
    "v_and_b32 v11, 1, v5\n"
    "v_lshl_add_u32 v10, v11, 5, v10\n"
    "v_and_b32 v11, 2, v5\n"
    "v_lshl_add_u32 v10, v11, 2, v10\n"
    "s_lshl_b32 s3, s27, 14\n"
    "s_lshl_b32 s54, s36, 8\n"
    "s_add_u32 s3, s3, s54\n"
    "s_add_u32 s3, s3, 116736\n"
    "v_lshl_add_u32 v9, v5, 4, s3\n"
    "v_lshl_add_u32 v9, v6, 10, v9\n"
    "s_mov_b32 s59, 0x1000\n"
    "s_mov_b32 s60, 0x2000\n"
    "s_mov_b32 s61, 0x3000\n"
    "v_mov_b32 v24, -1\n"
    "v_mov_b32 v25, -1\n"
    "v_mov_b32 v26, -1\n"
    "v_mov_b32 v27, -1\n"
    "v_mov_b32 v28, -1\n"
    "v_mov_b32 v29, -1\n"
    "v_mov_b32 v30, -1\n"
    "v_mov_b32 v31, -1\n"
    "v_mov_b32 v32, -1\n"
    "v_mov_b32 v33, -1\n"
    "v_mov_b32 v34, -1\n"
    "v_mov_b32 v35, -1\n"
    "v_mov_b32 v36, -1\n"
    "v_mov_b32 v37, -1\n"
    "v_mov_b32 v38, -1\n"
    "v_mov_b32 v39, -1\n"
    "s_mov_b32 s72, 0\n"
    "s_branch .Lkfw75\n"
    ".Lkfw74:\n"
    "s_sleep 2\n"
    "s_cmp_eq_u32 s38, 0\n"
    "s_cbranch_scc1 .Lkfw76\n"
    "s_sub_u32 s38, s38, 1\n"
    ".Lkfw75:\n"
    "ds_read_b32 v16, v125 offset:636\n"
    "s_waitcnt lgkmcnt(0)\n"
    "v_cmp_eq_u32 vcc, 0, v16\n"
    "s_cbranch_vccnz .Lkfw74\n"
    ".Lkfw76:\n"
    "s_cbranch_execz .Lk_wm2\n"
    "s_branch .Lk_wm1\n"
    ".Lk_wm0:\n"
    "s_mov_b64 s[62:63], vcc\n"
    "s_branch .Lkfw78\n"
    ".Lkfw77:\n"
    "s_sleep 1\n"
    "s_cmp_eq_u32 s38, 0\n"
    "s_cbranch_scc1 .Lkfw79\n"
    "s_sub_u32 s38, s38, 1\n"
    ".Lkfw78:\n"
    "ds_read_b32 v16, v125 offset:60\n"
    "s_waitcnt lgkmcnt(0)\n"
    "v_cmp_eq_u32 vcc, 0, v16\n"
    "s_cbranch_vccnz .Lkfw77\n"
    ".Lkfw79:\n"
    "s_sleep 8\n"
    "s_cmp_eq_u32 s38, 0\n"
    "s_cbranch_scc1 .Lk_wm2\n"
    "s_sub_u32 s38, s38, 1\n"
    "s_mov_b64 exec, s[62:63]\n"
    ".Lk_wm1:\n"
    "buffer_load_dwordx4 v[14:17], v8, s[40:43], 0 offen sc1\n"
    "s_mov_b64 exec, -1\n"
    "s_waitcnt vmcnt(0)\n"
    "v_max_u32 v16, v14, v15\n"
    "v_cmp_eq_u32 vcc, -1, v16\n"
    "s_cbranch_vccnz .Lk_wm0\n"
    ".Lk_wm2:\n"
    "v_mov_b32 v20, v14\n"
    "s_nop 1\n"
    "v_max_f32_dpp v20, v20, v20 row_ror:1 row_mask:0xf bank_mask:0xf\n"
    "s_nop 1\n"
    "v_max_f32_dpp v20, v20, v20 row_ror:2 row_mask:0xf bank_mask:0xf\n"
    "s_nop 1\n"
    "v_max_f32_dpp v20, v20, v20 row_ror:4 row_mask:0xf bank_mask:0xf\n"
    "s_nop 1\n"
    "v_max_f32_dpp v20, v20, v20 row_ror:8 row_mask:0xf bank_mask:0xf\n"
    "v_sub_f32 v20, v14, v20\n"
    "v_mul_f32 v20, 0x3fb8aa3b, v20\n"
    "v_exp_f32 v20, v20\n"
    "s_nop 0\n"
    "v_mul_f32 v21, v20, v15\n"
    "s_nop 1\n"
    "v_add_f32_dpp v21, v21, v21 row_ror:1 row_mask:0xf bank_mask:0xf\n"
    "s_nop 1\n"
    "v_add_f32_dpp v21, v21, v21 row_ror:2 row_mask:0xf bank_mask:0xf\n"
    "s_nop 1\n"
    "v_add_f32_dpp v21, v21, v21 row_ror:4 row_mask:0xf bank_mask:0xf\n"
    "s_nop 1\n"
    "v_add_f32_dpp v21, v21, v21 row_ror:8 row_mask:0xf bank_mask:0xf\n"
    "v_rcp_f32 v21, v21\n"
    "s_nop 0\n"
    "v_mul_f32 v20, v20, v21\n"
    "s_nop 1\n"
    "v_mov_b32_dpp v16, v20 row_newbcast:0 row_mask:0xf bank_mask:0xf\n"
    "v_mov_b32_dpp v17, v20 row_newbcast:4 row_mask:0xf bank_mask:0xf\n"
    "v_mov_b32_dpp v18, v20 row_newbcast:8 row_mask:0xf bank_mask:0xf\n"
    "v_mov_b32_dpp v19, v20 row_newbcast:12 row_mask:0xf bank_mask:0xf\n"
    "s_branch .Lkfw81\n"
    ".Lkfw80:\n"
    "s_cmp_eq_u32 s38, 0\n"
    "s_cbranch_scc1 .Lkfw82\n"
    "s_sub_u32 s38, s38, 1\n"
    ".Lkfw81:\n"
    "ds_read_b32 v20, v125 offset:60\n"
    "s_waitcnt lgkmcnt(0)\n"
    "v_cmp_eq_u32 vcc, 0, v20\n"
    "s_cbranch_vccnz .Lkfw80\n"
    ".Lkfw82:\n"
    "s_cbranch_execz .Lk_wo1\n"
    "s_sleep 4\n"
    "s_add_u32 s80, s80, 22\n"
    "s_mul_i32 s80, s80, 930\n"
    "s_ashr_i32 s80, s80, 8\n"
    "s_branch .Lkut84\n"
    ".Lkut83:\n"
    "s_sleep 1\n"
    ".Lkut84:\n"
    "s_memrealtime s[76:77]\n"
    "s_waitcnt lgkmcnt(0)\n"
    "s_sub_u32 s76, s76, s74\n"
    "s_cmp_lt_i32 s76, s80\n"
    "s_cbranch_scc1 .Lkut83\n"
    ".Lk_wall:\n"
    "buffer_load_dwordx4 v[24:27], v9, s[40:43], 0 offen sc1\n"
    "buffer_load_dwordx4 v[28:31], v9, s[40:43], s59 offen sc1\n"
    "buffer_load_dwordx4 v[32:35], v9, s[40:43], s60 offen sc1\n"
    "buffer_load_dwordx4 v[36:39], v9, s[40:43], s61 offen sc1\n"
    "s_waitcnt vmcnt(0)\n"
    "s_branch .Lk_wo1\n"
    ".Lk_wo0:\n"
    "s_cmp_eq_u32 s38, 0\n"
    "s_cbranch_scc1 .Lk_wo2\n"
    "s_sub_u32 s38, s38, 1\n"
    "v_cmp_eq_u32 s[62:63], -1, v27\n"
    "v_cmp_eq_u32 s[64:65], -1, v31\n"
    "v_cmp_eq_u32 s[66:67], -1, v35\n"
    "v_cmp_eq_u32 s[68:69], -1, v39\n"
    "s_mov_b64 exec, s[62:63]\n"
    "buffer_load_dwordx4 v[24:27], v9, s[40:43], 0 offen sc1\n"
    "s_mov_b64 exec, s[64:65]\n"
    "buffer_load_dwordx4 v[28:31], v9, s[40:43], s59 offen sc1\n"
    "s_mov_b64 exec, s[66:67]\n"
    "buffer_load_dwordx4 v[32:35], v9, s[40:43], s60 offen sc1\n"
    "s_mov_b64 exec, s[68:69]\n"
    "buffer_load_dwordx4 v[36:39], v9, s[40:43], s61 offen sc1\n"
    "s_mov_b64 exec, -1\n"
    "s_waitcnt vmcnt(8)\n"
    ".Lk_wo1:\n"
    "v_max3_u32 v20, v27, v31, v35\n"
    "v_max_u32 v20, v20, v39\n"
    "v_cmp_eq_u32 vcc, -1, v20\n"
    "s_cbranch_vccnz .Lk_wo0\n"
    ".Lk_wo2:\n"
    "v_pk_mul_f32 v[40:41], v[24:25], v[16:17] op_sel:[0,0] op_sel_hi:[1,0]\n"
    "v_pk_mul_f32 v[42:43], v[26:27], v[16:17] op_sel:[0,0] op_sel_hi:[1,0]\n"
    "v_pk_fma_f32 v[40:41], v[28:29], v[16:17], v[40:41] op_sel:[0,1,0] op_sel_hi:[1,1,1]\n"
    "v_pk_fma_f32 v[42:43], v[30:31], v[16:17], v[42:43] op_sel:[0,1,0] op_sel_hi:[1,1,1]\n"
    "v_pk_fma_f32 v[40:41], v[32:33], v[18:19], v[40:41] op_sel:[0,0,0] op_sel_hi:[1,0,1]\n"
    "v_pk_fma_f32 v[42:43], v[34:35], v[18:19], v[42:43] op_sel:[0,0,0] op_sel_hi:[1,0,1]\n"
    "v_pk_fma_f32 v[40:41], v[36:37], v[18:19], v[40:41] op_sel:[0,1,0] op_sel_hi:[1,1,1]\n"
    "v_pk_fma_f32 v[42:43], v[38:39], v[18:19], v[42:43] op_sel:[0,1,0] op_sel_hi:[1,1,1]\n"
    "s_nop 1\n"
    "v_permlane32_swap_b32 v40, v42\n"
    "v_permlane32_swap_b32 v41, v43\n"
    "v_pk_add_f32 v[40:41], v[40:41], v[42:43]\n"
    "s_nop 1\n"
    "v_permlane16_swap_b32 v40, v41\n"
    "v_add_f32 v40, v40, v41\n"
    "v_cmp_u_f32 vcc, v40, v40\n"
    "v_cvt_pk_bf16_f32 v40, v40, v40\n"
    "ds_write_b16 v10, v40 offset:2880\n"
    "s_cbranch_vccnz .Lk_wnan\n"
    ".Lk_wok:\n"
    "s_cbranch_execz .Lk_end\n"
    "s_waitcnt lgkmcnt(0)\n"
    "s_barrier\n"
    "s_barrier\n"
    "s_branch .Lk_end\n"
    ".Lk_wnan:\n"
    "s_waitcnt vmcnt(0)\n"
    "v_mov_b32 v20, 0\n"
    "v_max3_u32 v20, v20, v24, v25\n"
    "v_max3_u32 v20, v20, v26, v27\n"
    "v_max3_u32 v20, v20, v28, v29\n"
    "v_max3_u32 v20, v20, v30, v31\n"
    "v_max3_u32 v20, v20, v32, v33\n"
    "v_max3_u32 v20, v20, v34, v35\n"
    "v_max3_u32 v20, v20, v36, v37\n"
    "v_max3_u32 v20, v20, v38, v39\n"
    "v_cmp_eq_u32 vcc, -1, v20\n"
    "s_cbranch_vccnz .Lknp85\n"
    "s_cmp_lg_u32 s72, 0\n"
    "s_mov_b32 s72, 1\n"
    "s_cbranch_scc1 .Lk_wok\n"
    "s_branch .Lk_wo2\n"
    ".Lknp85:\n"
    "s_cmp_eq_u32 s38, 0\n"
    "s_cbranch_scc1 .Lk_wok\n"
    "s_sub_u32 s38, s38, 1\n"
    "s_branch .Lk_wall\n"
    ".Lk_st1:\n"
    "s_cmp_eq_u32 s36, 3\n"
    "s_cbranch_scc1 .Lk_w2b\n"
    "s_mov_b64 s[48:49], s[16:17]\n"
    "s_mov_b32 s50, 1152\n"
    "s_mov_b64 s[44:45], s[22:23]\n"
    "s_mov_b32 s46, 5308416\n"
    "s_mul_i32 s55, s35, 73728\n"
    "s_mul_i32 s3, s36, 36848\n"
    "s_add_u32 s55, s55, s3\n"
    "s_mul_i32 s3, s36, 192\n"
    "v_lshl_add_u32 v8, v5, 3, s3\n"
    "v_and_b32 v9, 2, v6\n"
    "v_lshl_add_u32 v8, v9, 6, v8\n"
    "v_and_b32 v9, 1, v6\n"
    "v_lshl_add_u32 v8, v9, 2, v8\n"
    "v_and_b32 v9, 40, v1\n"
    "v_cmp_ne_u32 s[56:57], 40, v9\n"
    "v_mov_b32 v10, 0x7ffffff0\n"
    "v_lshlrev_b32 v9, 1, v8\n"
    "v_cndmask_b32 v9, v10, v9, s[56:57]\n"
    "v_lshlrev_b32 v11, 2, v8\n"
    "v_add_u32 v11, 5632, v11\n"
    "v_cndmask_b32 v11, v10, v11, s[56:57]\n"
    "s_mov_b32 s59, 2304\n"
    "s_mov_b32 s60, 4608\n"
    "s_cbranch_execz .Lk_m2s\n"
    "buffer_load_dwordx4 v[28:31], off, s[44:47], s55\n"
    "buffer_load_dwordx4 v[24:27], v9, s[48:51], 0 offen\n"
    ".Lk_m20:\n"
    "buffer_load_dwordx4 v[12:15], v11, s[40:43], 0 offen sc1\n"
    "buffer_load_dwordx4 v[16:19], v11, s[40:43], s59 offen sc1\n"
    "buffer_load_dwordx4 v[20:23], v11, s[40:43], s60 offen sc1\n"
    ".Lk_m2s:\n"
    "v_lshrrev_b32 v73, 5, v8\n"
    "v_lshlrev_b32 v73, 6, v73\n"
    "v_and_b32 v32, 12, v8\n"
    "v_lshl_add_u32 v73, v32, 2, v73\n"
    "v_and_b32 v32, 16, v8\n"
    "v_lshrrev_b32 v32, 1, v32\n"
    "v_add_u32 v73, v73, v32\n"
    "v_mov_b32 v32, 1152\n"
    "v_cndmask_b32 v73, v32, v73, s[56:57]\n"
    "s_lshl_b32 s54, s3, 1\n"
    "s_add_u32 s54, s54, 32768\n"
    "v_lshl_add_u32 v72, v5, 4, s54\n"
    "v_mul_u32_u24 v32, 1152, v6\n"
    "v_add_u32 v72, v72, v32\n"
    "v_add_u32 v74, 256, v72\n"
    "v_cmp_gt_u32 vcc, 8, v5\n"
    "s_nop 1\n"
    "v_cndmask_b32 v74, v10, v74, vcc\n"
    "v_and_b32 v76, 3, v1\n"
    "v_lshlrev_b32 v76, 2, v76\n"
    "v_lshrrev_b32 v75, 3, v1\n"
    "v_and_b32 v75, 4, v75\n"
    "v_lshl_add_u32 v75, v2, 2, v75\n"
    "v_and_b32 v32, 1, v6\n"
    "v_lshl_add_u32 v75, v32, 3, v75\n"
    "s_waitcnt vmcnt(0)\n"
    "v_cmp_ne_u32 vcc, v13, v7\n"
    "v_cmp_ne_u32 s[54:55], v15, v7\n"
    "s_or_b64 vcc, vcc, s[54:55]\n"
    "v_cmp_ne_u32 s[54:55], v17, v7\n"
    "s_or_b64 vcc, vcc, s[54:55]\n"
    "v_cmp_ne_u32 s[54:55], v19, v7\n"
    "s_or_b64 vcc, vcc, s[54:55]\n"
    "v_cmp_ne_u32 s[54:55], v21, v7\n"
    "s_or_b64 vcc, vcc, s[54:55]\n"
    "v_cmp_ne_u32 s[54:55], v23, v7\n"
    "s_or_b64 vcc, vcc, s[54:55]\n"
    "s_and_b64 vcc, vcc, s[56:57]\n"
    "s_cbranch_vccz .Lk_m21\n"
    "s_cmp_eq_u32 s38, 0\n"
    "s_cbranch_scc1 .Lk_m21\n"
    "s_sub_u32 s38, s38, 1\n"
    "s_branch .Lk_m20\n"
    ".Lk_m21:\n"
    "v_lshlrev_b32 v56, 16, v12\n"
    "v_and_b32 v57, 0xffff0000, v12\n"
    "v_lshlrev_b32 v60, 16, v24\n"
    "v_and_b32 v61, 0xffff0000, v24\n"
    "v_lshlrev_b32 v32, 16, v20\n"
    "v_and_b32 v33, 0xffff0000, v20\n"
    "v_add_f32 v32, 1.0, v32\n"
    "v_add_f32 v33, 1.0, v33\n"
    "v_cvt_pk_bf16_f32 v34, v32, v33\n"
    "v_lshlrev_b32 v32, 16, v34\n"
    "v_and_b32 v33, 0xffff0000, v34\n"
    "v_pk_mul_f32 v[64:65], v[56:57], v[32:33]\n"
    "v_pk_mul_f32 v[68:69], v[60:61], v[32:33]\n"
    "v_lshlrev_b32 v58, 16, v14\n"
    "v_and_b32 v59, 0xffff0000, v14\n"
    "v_lshlrev_b32 v62, 16, v25\n"
    "v_and_b32 v63, 0xffff0000, v25\n"
    "v_lshlrev_b32 v32, 16, v22\n"
    "v_and_b32 v33, 0xffff0000, v22\n"
    "v_add_f32 v32, 1.0, v32\n"
    "v_add_f32 v33, 1.0, v33\n"
    "v_cvt_pk_bf16_f32 v35, v32, v33\n"
    "v_lshlrev_b32 v32, 16, v35\n"
    "v_and_b32 v33, 0xffff0000, v35\n"
    "v_pk_mul_f32 v[66:67], v[58:59], v[32:33]\n"
    "v_pk_mul_f32 v[70:71], v[62:63], v[32:33]\n"
    "ds_write_b64 v73, v[34:35] offset:5312\n"
    "v_mov_b32 v17, v18\n"
    "ds_write_b64 v73, v[16:17] offset:6528\n"
    "v_mov_b32 v32, 0x3f80\n"
    "v_mov_b32 v33, 0x3f800000\n"
    "v_and_b32 v36, 1, v1\n"
    "v_cmp_eq_u32 vcc, 0, v36\n"
    "s_nop 1\n"
    "v_cndmask_b32 v32, v33, v32, vcc\n"
    "v_lshrrev_b32 v33, 1, v5\n"
    "v_cmp_eq_u32 vcc, 0, v33\n"
    "s_nop 1\n"
    "v_cndmask_b32 v40, 0, v32, vcc\n"
    "v_cmp_eq_u32 vcc, 1, v33\n"
    "s_nop 1\n"
    "v_cndmask_b32 v41, 0, v32, vcc\n"
    "v_cmp_eq_u32 vcc, 2, v33\n"
    "s_nop 1\n"
    "v_cndmask_b32 v42, 0, v32, vcc\n"
    "v_cmp_eq_u32 vcc, 3, v33\n"
    "s_nop 1\n"
    "v_cndmask_b32 v43, 0, v32, vcc\n"
    "v_cmp_eq_u32 vcc, 4, v33\n"
    "s_nop 1\n"
    "v_cndmask_b32 v44, 0, v32, vcc\n"
    "v_cmp_eq_u32 vcc, 5, v33\n"
    "s_nop 1\n"
    "v_cndmask_b32 v45, 0, v32, vcc\n"
    "v_cmp_eq_u32 vcc, 6, v33\n"
    "s_nop 1\n"
    "v_cndmask_b32 v46, 0, v32, vcc\n"
    "v_cmp_eq_u32 vcc, 7, v33\n"
    "s_nop 1\n"
    "v_cndmask_b32 v47, 0, v32, vcc\n"
    "s_mov_b32 s59, 4608\n"
    "s_mov_b32 s60, 9216\n"
    "s_mov_b32 s61, 13824\n"
    "s_mov_b32 s70, 0x07060302\n"
    "s_waitcnt lgkmcnt(0)\n"
    "s_cbranch_execz .Lk_b1\n"
    "s_add_u32 s80, s80, 54\n"
    "s_mul_i32 s80, s80, 930\n"
    "s_ashr_i32 s80, s80, 8\n"
    "v_readfirstlane_b32 s58, v7\n"
    "s_add_u32 s54, s14, 851968\n"
    "s_addc_u32 s55, s15, 0\n"
    "s_add_u32 s83, s80, 240\n"
    ".Lknt86:\n"
    "s_sub_u32 s82, s80, 62\n"
    "s_branch .Lkut89\n"
    ".Lkut88:\n"
    "s_sleep 1\n"
    ".Lkut89:\n"
    "s_memrealtime s[76:77]\n"
    "s_waitcnt lgkmcnt(0)\n"
    "s_sub_u32 s76, s76, s74\n"
    "s_cmp_lt_i32 s76, s82\n"
    "s_cbranch_scc1 .Lkut88\n"
    "s_load_dword s62, s[54:55], 0x0\n"
    "s_load_dword s63, s[54:55], 0x80\n"
    "s_load_dword s64, s[54:55], 0x100\n"
    "s_load_dword s65, s[54:55], 0x180\n"
    "s_add_u32 s54, s54, 512\n"
    "s_addc_u32 s55, s55, 0\n"
    "s_waitcnt lgkmcnt(0)\n"
    "s_xor_b32 s62, s62, s58\n"
    "s_xor_b32 s63, s63, s58\n"
    "s_xor_b32 s64, s64, s58\n"
    "s_xor_b32 s65, s65, s58\n"
    "s_min_u32 s62, s62, s63\n"
    "s_min_u32 s64, s64, s65\n"
    "s_min_u32 s62, s62, s64\n"
    "s_cmp_lg_u32 s62, 0\n"
    "s_cbranch_scc1 .Lknt87\n"
    "s_add_u32 s80, s80, 30\n"
    "s_cmp_lt_i32 s80, s83\n"
    "s_cbranch_scc1 .Lknt86\n"
    ".Lknt87:\n"
    "s_branch .Lkfw91\n"
    ".Lkfw90:\n"
    "s_sleep 1\n"
    "s_cmp_eq_u32 s38, 0\n"
    "s_cbranch_scc1 .Lkfw92\n"
    "s_sub_u32 s38, s38, 1\n"
    ".Lkfw91:\n"
    "ds_read_b32 v77, v76 offset:112\n"
    "s_waitcnt lgkmcnt(0)\n"
    "v_cmp_eq_u32 vcc, 0, v77\n"
    "s_cbranch_vccnz .Lkfw90\n"
    ".Lkfw92:\n"
    "s_branch .Lkut94\n"
    ".Lkut93:\n"
    "s_sleep 1\n"
    ".Lkut94:\n"
    "s_memrealtime s[76:77]\n"
    "s_waitcnt lgkmcnt(0)\n"
    "s_sub_u32 s76, s76, s74\n"
    "s_cmp_lt_i32 s76, s80\n"
    "s_cbranch_scc1 .Lkut93\n"
    "buffer_load_dwordx4 v[8:11], v72, s[40:43], 0 offen sc1\n"
    "buffer_load_dwordx4 v[12:15], v74, s[40:43], 0 offen sc1\n"
    "buffer_load_dwordx4 v[16:19], v72, s[40:43], s59 offen sc1\n"
    "buffer_load_dwordx4 v[20:23], v74, s[40:43], s59 offen sc1\n"
    "buffer_load_dwordx4 v[24:27], v72, s[40:43], s60 offen sc1\n"
    "buffer_load_dwordx4 v[28:31], v74, s[40:43], s60 offen sc1\n"
    "buffer_load_dwordx4 v[32:35], v72, s[40:43], s61 offen sc1\n"
    "buffer_load_dwordx4 v[36:39], v74, s[40:43], s61 offen sc1\n"
    "ds_write_b32 v2, v124 offset:96\n"
    "s_branch .Lk_b1\n"
    ".Lk_b0:\n"
    "s_mov_b32 s72, 0\n"
    ".Lk_b3:\n"
    "v_max3_u32 v77, v8, v9, v10\n"
    "v_max3_u32 v78, v11, v12, v13\n"
    "v_max3_u32 v77, v77, v14, v15\n"
    "v_max_u32 v77, v77, v78\n"
    "v_cmp_eq_u32 s[62:63], -1, v77\n"
    "v_max3_u32 v77, v16, v17, v18\n"
    "v_max3_u32 v78, v19, v20, v21\n"
    "v_max3_u32 v77, v77, v22, v23\n"
    "v_max_u32 v77, v77, v78\n"
    "v_cmp_eq_u32 s[64:65], -1, v77\n"
    "v_max3_u32 v77, v24, v25, v26\n"
    "v_max3_u32 v78, v27, v28, v29\n"
    "v_max3_u32 v77, v77, v30, v31\n"
    "v_max_u32 v77, v77, v78\n"
    "v_cmp_eq_u32 s[66:67], -1, v77\n"
    "v_max3_u32 v77, v32, v33, v34\n"
    "v_max3_u32 v78, v35, v36, v37\n"
    "v_max3_u32 v77, v77, v38, v39\n"
    "v_max_u32 v77, v77, v78\n"
    "v_cmp_eq_u32 s[68:69], -1, v77\n"
    "s_or_b64 s[54:55], s[62:63], s[64:65]\n"
    "s_or_b64 s[54:55], s[54:55], s[66:67]\n"
    "s_or_b64 s[54:55], s[54:55], s[68:69]\n"
    "s_cbranch_scc0 .Lk_b4\n"
    "s_mov_b64 exec, s[62:63]\n"
    "buffer_load_dwordx4 v[8:11], v72, s[40:43], 0 offen sc1\n"
    "buffer_load_dwordx4 v[12:15], v74, s[40:43], 0 offen sc1\n"
    "s_mov_b64 exec, s[64:65]\n"
    "buffer_load_dwordx4 v[16:19], v72, s[40:43], s59 offen sc1\n"
    "buffer_load_dwordx4 v[20:23], v74, s[40:43], s59 offen sc1\n"
    "s_mov_b64 exec, s[66:67]\n"
    "buffer_load_dwordx4 v[24:27], v72, s[40:43], s60 offen sc1\n"
    "buffer_load_dwordx4 v[28:31], v74, s[40:43], s60 offen sc1\n"
    "s_mov_b64 exec, s[68:69]\n"
    "buffer_load_dwordx4 v[32:35], v72, s[40:43], s61 offen sc1\n"
    "buffer_load_dwordx4 v[36:39], v74, s[40:43], s61 offen sc1\n"
    "s_mov_b64 exec, -1\n"
    "s_mov_b32 s72, 1\n"
    "s_waitcnt vmcnt(0)\n"
    "s_cmp_eq_u32 s38, 0\n"
    "s_cbranch_scc1 .Lk_b1\n"
    "s_sub_u32 s38, s38, 1\n"
    "s_branch .Lk_b3\n"
    ".Lk_b4:\n"
    "s_cmp_eq_u32 s72, 0\n"
    "s_cbranch_scc1 .Lk_bok\n"
    ".Lk_b1:\n"
    "s_waitcnt vmcnt(7)\n"
    "v_mfma_f32_16x16x32_bf16 v[80:83], v[40:43], v[8:11], 0\n"
    "s_waitcnt vmcnt(6)\n"
    "v_mfma_f32_16x16x32_bf16 v[80:83], v[44:47], v[12:15], v[80:83]\n"
    "s_waitcnt vmcnt(5)\n"
    "v_mfma_f32_16x16x32_bf16 v[80:83], v[40:43], v[16:19], v[80:83]\n"
    "s_waitcnt vmcnt(4)\n"
    "v_mfma_f32_16x16x32_bf16 v[80:83], v[44:47], v[20:23], v[80:83]\n"
    "s_waitcnt vmcnt(3)\n"
    "v_mfma_f32_16x16x32_bf16 v[80:83], v[40:43], v[24:27], v[80:83]\n"
    "s_waitcnt vmcnt(2)\n"
    "v_mfma_f32_16x16x32_bf16 v[80:83], v[44:47], v[28:31], v[80:83]\n"
    "s_waitcnt vmcnt(1)\n"
    "v_mfma_f32_16x16x32_bf16 v[80:83], v[40:43], v[32:35], v[80:83]\n"
    "s_waitcnt vmcnt(0)\n"
    "v_mfma_f32_16x16x32_bf16 v[80:83], v[44:47], v[36:39], v[80:83]\n"
    "s_nop 7\n"
    "v_pk_fma_f32 v[84:85], v[80:81], v[56:57], v[60:61]\n"
    "v_pk_fma_f32 v[86:87], v[82:83], v[58:59], v[62:63]\n"
    "v_pk_fma_f32 v[80:81], v[80:81], v[64:65], v[68:69]\n"
    "v_pk_add_f32 v[92:93], v[84:85], v[86:87]\n"
    "v_pk_mul_f32 v[84:85], v[84:85], v[84:85]\n"
    "v_pk_fma_f32 v[82:83], v[82:83], v[66:67], v[70:71]\n"
    "v_pk_fma_f32 v[84:85], v[86:87], v[86:87], v[84:85]\n"
    "v_add_f32 v92, v92, v93\n"
    "v_add_f32 v93, v84, v85\n"
    "v_perm_b32 v88, v81, v80, s70\n"
    "v_perm_b32 v89, v83, v82, s70\n"
    "v_permlane32_swap_b32 v92, v93\n"
    "ds_write_b64 v73, v[88:89] offset:2880\n"
    "v_add_f32 v92, v92, v93\n"
    "v_and_b32 v84, 0xffff0000, v80\n"
    "v_and_b32 v85, 0xffff0000, v81\n"
    "v_add_f32_dpp v92, v92, v92 row_ror:1 row_mask:0xf bank_mask:0xf\n"
    "v_and_b32 v86, 0xffff0000, v82\n"
    "v_and_b32 v87, 0xffff0000, v83\n"
    "v_add_f32_dpp v92, v92, v92 row_ror:2 row_mask:0xf bank_mask:0xf\n"
    "v_pk_add_f32 v[80:81], v[80:81], v[84:85] neg_lo:[0,1] neg_hi:[0,1]\n"
    "v_pk_add_f32 v[82:83], v[82:83], v[86:87] neg_lo:[0,1] neg_hi:[0,1]\n"
    "v_add_f32_dpp v92, v92, v92 row_ror:4 row_mask:0xf bank_mask:0xf\n"
    "v_perm_b32 v90, v81, v80, s70\n"
    "v_perm_b32 v91, v83, v82, s70\n"
    "v_add_f32_dpp v92, v92, v92 row_ror:8 row_mask:0xf bank_mask:0xf\n"
    "ds_write_b64 v73, v[90:91] offset:4096\n"
    "ds_write_b32 v75, v92 offset:768\n"
    "v_cmp_u_f32 vcc, v92, v92\n"
    "s_cbranch_vccnz .Lk_b5\n"
    ".Lk_bok:\n"
    "s_cbranch_execz .Lk_w2b\n"
    "s_waitcnt lgkmcnt(0)\n"
    "s_barrier\n"
    "s_barrier\n"
    "s_branch .Lk_end\n"
    ".Lk_b5:\n"
    "s_cmp_eq_u32 s38, 0\n"
    "s_cbranch_scc1 .Lk_bok\n"
    "s_sub_u32 s38, s38, 1\n"
    "s_branch .Lk_b0\n"
    ".Lk_w2b:\n"
    "v_lshrrev_b32 v9, 3, v1\n"
    "v_and_b32 v8, 6, v9\n"
    "v_and_b32 v10, 7, v1\n"
    "v_xor_b32 v8, v8, v10\n"
    "v_lshlrev_b32 v8, 4, v8\n"
    "s_mov_b64 s[44:45], s[22:23]\n"
    "s_mov_b32 s46, 5308416\n"
    "s_mul_i32 s54, s35, 73728\n"
    "s_add_u32 s54, s54, 55296\n"
    "v_mul_u32_u24 v9, 1152, v9\n"
    "v_add3_u32 v8, v8, v9, s54\n"
    "s_mov_b32 m0, 30720\n"
    "s_movk_i32 s56, 0x2000\n"
    "s_mov_b32 s57, 0\n"
    "v_min_u32 v9, 2, v1\n"
    "v_lshlrev_b32 v9, 2, v9\n"
    "s_branch .Lkfw96\n"
    ".Lkfw95:\n"
    "s_sleep 4\n"
    "s_cmp_eq_u32 s38, 0\n"
    "s_cbranch_scc1 .Lkfw97\n"
    "s_sub_u32 s38, s38, 1\n"
    ".Lkfw96:\n"
    "ds_read_b32 v10, v125 offset:112\n"
    "s_waitcnt lgkmcnt(0)\n"
    "v_cmp_eq_u32 vcc, 0, v10\n"
    "s_cbranch_vccnz .Lkfw95\n"
    ".Lkfw97:\n"
    "s_branch .Lkfw99\n"
    ".Lkfw98:\n"
    "s_cmp_eq_u32 s38, 0\n"
    "s_cbranch_scc1 .Lkfw100\n"
    "s_sub_u32 s38, s38, 1\n"
    ".Lkfw99:\n"
    "ds_read_b32 v10, v9 offset:96\n"
    "s_waitcnt lgkmcnt(0)\n"
    "v_cmp_eq_u32 vcc, 0, v10\n"
    "s_cbranch_vccnz .Lkfw98\n"
    ".Lkfw100:\n"
    "s_cbranch_execz .Lk_end\n"
    ".Lk_w2c:\n"
    "buffer_load_dwordx4 v8, s[44:47], s57 offen nt lds\n"
    "buffer_load_dwordx4 v8, s[44:47], s56 offen offset:1024 nt lds\n"
    "s_addk_i32 s56, 128\n"
    "s_add_u32 m0, m0, 0x2000\n"
    "s_addk_i32 s57, 128\n"
    "s_cmpk_lg_u32 s57, 1152\n"
    "s_cbranch_scc1 .Lk_w2c\n"
    "s_barrier\n"
    "s_waitcnt vmcnt(0)\n"
    "ds_write_b32 v125, v124 offset:32\n"
    "s_barrier\n"
    "s_branch .Lk_end\n"
    ".Lk_fin:\n"
    "s_cmp_lg_u32 s36, 2\n"
    "s_cbranch_scc1 .Lk_end\n"
    "s_bitcmp1_b32 s28, 31\n"
    "s_cbranch_scc0 .Lk_end\n"
    "v_lshrrev_b32 v6, 4, v1\n"
    "ds_read_b32 v7, v125 offset:52\n"
    "s_waitcnt lgkmcnt(0)\n"
    "s_bfe_u32 s57, s28, 0x60015\n"
    "s_lshl_b32 s3, s57, 2\n"
    "v_and_b32 v8, 3, v1\n"
    "v_add_u32 v8, s3, v8\n"
    "s_mul_i32 s3, s57, 7282\n"
    "s_lshr_b32 s3, s3, 16\n"
    "s_mul_i32 s54, s3, 9\n"
    "s_sub_u32 s54, s57, s54\n"
    "s_lshl_b32 s54, s54, 6\n"
    "s_lshl_b32 s3, s3, 2\n"
    "v_mov_b32 v9, s3\n"
    "s_lshl_b32 s3, s3, 2\n"
    "s_add_u32 s3, s3, s54\n"
    "v_and_b32 v10, 3, v1\n"
    "v_lshl_add_u32 v10, v10, 2, s3\n"
    "v_add_u32 v11, v10, v6\n"
    "v_lshrrev_b32 v12, 1, v11\n"
    "v_lshlrev_b32 v12, 3, v12\n"
    "v_add_u32 v12, 5632, v12\n"
    "v_lshlrev_b32 v11, 1, v11\n"
    "s_mov_b64 s[48:49], s[16:17]\n"
    "s_mov_b32 s50, 1152\n"
    "s_mov_b32 s59, 6912\n"
    "s_mov_b32 s60, 32768\n"
    "s_mov_b32 s61, 378880\n"
    "v_lshrrev_b32 v19, 2, v9\n"
    "v_lshlrev_b32 v19, 6, v19\n"
    "v_lshl_add_u32 v19, v8, 4, v19\n"
    "v_lshrrev_b32 v9, 2, v1\n"
    "v_mul_u32_u24 v18, 1152, v9\n"
    "v_lshl_add_u32 v18, v10, 1, v18\n"
    "v_add_u32 v18, s60, v18\n"
    "v_mul_u32_u24 v9, 2560, v9\n"
    "v_add3_u32 v19, v19, v9, s61\n"
    "v_add_u32 v20, 163840, v19\n"
    "v_mov_b32 v9, 0x7ffffff0\n"
    "v_cmp_gt_u32 vcc, 32, v1\n"
    "s_nop 1\n"
    "v_cndmask_b32 v20, v9, v20, vcc\n"
    "v_and_b32 v8, 12, v1\n"
    "v_cmp_eq_u32 vcc, 0, v8\n"
    "s_nop 1\n"
    "v_cndmask_b32 v21, v9, v11, vcc\n"
    "v_and_b32 v22, 16, v1\n"
    "s_cbranch_execz .Lk_fg2\n"
    "buffer_load_ushort v13, v11, s[48:51], 0 offen\n"
    "s_branch .Lk_fg1\n"
    ".Lk_fg0:\n"
    "s_cmp_eq_u32 s38, 0\n"
    "s_cbranch_scc1 .Lk_fg2\n"
    "s_sub_u32 s38, s38, 1\n"
    ".Lk_fg1:\n"
    "buffer_load_dwordx2 v[14:15], v12, s[40:43], 0 offen sc1\n"
    "buffer_load_dwordx2 v[16:17], v12, s[40:43], s59 offen sc1\n"
    "s_waitcnt vmcnt(0)\n"
    "v_cmp_ne_u32 vcc, v15, v7\n"
    "v_cmp_ne_u32 s[54:55], v17, v7\n"
    "s_or_b64 vcc, vcc, s[54:55]\n"
    "s_cbranch_vccnz .Lk_fg0\n"
    ".Lk_fg2:\n"
    "v_lshrrev_b32 v14, v22, v14\n"
    "v_lshrrev_b32 v16, v22, v16\n"
    "v_lshlrev_b32 v14, 16, v14\n"
    "v_lshlrev_b32 v16, 16, v16\n"
    "v_lshlrev_b32 v13, 16, v13\n"
    "s_mov_b64 s[48:49], s[24:25]\n"
    "s_mov_b32 s59, 40960\n"
    "s_mov_b32 s60, 81920\n"
    "s_mov_b32 s61, 122880\n"
    "s_cbranch_execz .Lk_fb2\n"
    "s_waitcnt vmcnt(0)\n"
    "s_add_u32 s80, s80, 50\n"
    "s_mul_i32 s80, s80, 930\n"
    "s_ashr_i32 s80, s80, 8\n"
    "v_readfirstlane_b32 s58, v7\n"
    "s_add_u32 s54, s14, 851968\n"
    "s_addc_u32 s55, s15, 0\n"
    "s_add_u32 s83, s80, 240\n"
    ".Lknt101:\n"
    "s_sub_u32 s82, s80, 62\n"
    "s_branch .Lkut104\n"
    ".Lkut103:\n"
    "s_sleep 1\n"
    ".Lkut104:\n"
    "s_memrealtime s[76:77]\n"
    "s_waitcnt lgkmcnt(0)\n"
    "s_sub_u32 s76, s76, s74\n"
    "s_cmp_lt_i32 s76, s82\n"
    "s_cbranch_scc1 .Lkut103\n"
    "s_load_dword s62, s[54:55], 0x0\n"
    "s_load_dword s63, s[54:55], 0x80\n"
    "s_load_dword s64, s[54:55], 0x100\n"
    "s_load_dword s65, s[54:55], 0x180\n"
    "s_add_u32 s54, s54, 512\n"
    "s_addc_u32 s55, s55, 0\n"
    "s_waitcnt lgkmcnt(0)\n"
    "s_xor_b32 s62, s62, s58\n"
    "s_xor_b32 s63, s63, s58\n"
    "s_xor_b32 s64, s64, s58\n"
    "s_xor_b32 s65, s65, s58\n"
    "s_min_u32 s62, s62, s63\n"
    "s_min_u32 s64, s64, s65\n"
    "s_min_u32 s62, s62, s64\n"
    "s_cmp_lg_u32 s62, 0\n"
    "s_cbranch_scc1 .Lknt102\n"
    "s_add_u32 s80, s80, 30\n"
    "s_cmp_lt_i32 s80, s83\n"
    "s_cbranch_scc1 .Lknt101\n"
    ".Lknt102:\n"
    "s_add_u32 s80, s80, 29\n"
    "s_branch .Lkut106\n"
    ".Lkut105:\n"
    "s_sleep 1\n"
    ".Lkut106:\n"
    "s_memrealtime s[76:77]\n"
    "s_waitcnt lgkmcnt(0)\n"
    "s_sub_u32 s76, s76, s74\n"
    "s_cmp_lt_i32 s76, s80\n"
    "s_cbranch_scc1 .Lkut105\n"
    "s_mov_b32 s58, 0\n"
    "s_branch .Lk_fb1\n"
    ".Lk_fb0:\n"
    "s_mov_b32 s58, 1\n"
    "s_cmp_eq_u32 s38, 0\n"
    "s_cbranch_scc1 .Lk_fb2\n"
    "s_sub_u32 s38, s38, 1\n"
    ".Lk_fb1:\n"
    "buffer_load_dwordx2 v[46:47], v18, s[40:43], 0 offen sc1\n"
    "s_waitcnt vmcnt(0)\n"
    "v_max_u32 v28, v46, v47\n"
    "v_cmp_eq_u32 vcc, -1, v28\n"
    "s_cbranch_vccnz .Lk_fb0\n"
    ".Lk_fb2:\n"
    "v_lshlrev_b32 v44, 16, v46\n"
    "v_and_b32 v45, 0xffff0000, v46\n"
    "v_lshlrev_b32 v46, 16, v47\n"
    "v_and_b32 v47, 0xffff0000, v47\n"
    "s_nop 0\n"
    "v_permlane32_swap_b32 v44, v46\n"
    "v_permlane32_swap_b32 v45, v47\n"
    "v_pk_add_f32 v[44:45], v[44:45], v[46:47]\n"
    "s_nop 1\n"
    "v_permlane16_swap_b32 v44, v45\n"
    "v_add_f32 v44, v44, v45\n"
    "s_nop 1\n"
    "v_add_f32_dpp v44, v44, v44 row_ror:4 row_mask:0xf bank_mask:0xf\n"
    "s_nop 1\n"
    "v_add_f32_dpp v44, v44, v44 row_ror:8 row_mask:0xf bank_mask:0xf\n"
    "v_cvt_pk_bf16_f32 v44, v44, v44\n"
    "v_lshlrev_b32 v44, 16, v44\n"
    "v_mul_f32 v44, v14, v44\n"
    "v_cvt_pk_bf16_f32 v44, v44, v44\n"
    "v_lshlrev_b32 v44, 16, v44\n"
    "v_add_f32 v13, v13, v44\n"
    "v_cvt_pk_bf16_f32 v13, v13, v13\n"
    "v_lshlrev_b32 v13, 16, v13\n"
    "v_mov_b32 v24, -1\n"
    "v_mov_b32 v25, -1\n"
    "v_mov_b32 v26, -1\n"
    "v_mov_b32 v27, -1\n"
    "v_mov_b32 v28, -1\n"
    "v_mov_b32 v29, -1\n"
    "v_mov_b32 v30, -1\n"
    "v_mov_b32 v31, -1\n"
    "v_mov_b32 v32, -1\n"
    "v_mov_b32 v33, -1\n"
    "v_mov_b32 v34, -1\n"
    "v_mov_b32 v35, -1\n"
    "v_mov_b32 v36, -1\n"
    "v_mov_b32 v37, -1\n"
    "v_mov_b32 v38, -1\n"
    "v_mov_b32 v39, -1\n"
    "v_mov_b32 v40, -1\n"
    "v_mov_b32 v41, -1\n"
    "v_mov_b32 v42, -1\n"
    "v_mov_b32 v43, -1\n"
    "s_mov_b32 s72, 0\n"
    "s_cbranch_execz .Lk_c1\n"
    "s_cmp_lg_u32 s57, 0\n"
    "s_cbranch_scc1 .Lk_fdone\n"
    "s_mov_b64 exec, 1\n"
    "buffer_store_dword v7, off, s[40:43], 0 offset:0 sc1\n"
    "s_mov_b64 exec, -1\n"
    ".Lk_fdone:\n"
    "s_cmp_eq_u32 s58, 0\n"
    "s_cbranch_scc1 .Lk_ct\n"
    "s_sleep 38\n"
    ".Lk_ct:\n"
    "s_add_u32 s80, s80, 104\n"
    "s_branch .Lkut108\n"
    ".Lkut107:\n"
    "s_sleep 1\n"
    ".Lkut108:\n"
    "s_memrealtime s[76:77]\n"
    "s_waitcnt lgkmcnt(0)\n"
    "s_sub_u32 s76, s76, s74\n"
    "s_cmp_lt_i32 s76, s80\n"
    "s_cbranch_scc1 .Lkut107\n"
    ".Lk_call:\n"
    "buffer_load_dwordx4 v[24:27], v19, s[40:43], 0 offen sc1\n"
    "buffer_load_dwordx4 v[28:31], v19, s[40:43], s59 offen sc1\n"
    "buffer_load_dwordx4 v[32:35], v19, s[40:43], s60 offen sc1\n"
    "buffer_load_dwordx4 v[36:39], v19, s[40:43], s61 offen sc1\n"
    "buffer_load_dwordx4 v[40:43], v20, s[40:43], 0 offen sc1\n"
    "s_branch .Lk_c1\n"
    ".Lk_c0:\n"
    "s_cmp_eq_u32 s38, 0\n"
    "s_cbranch_scc1 .Lk_c2\n"
    "s_sub_u32 s38, s38, 1\n"
    "v_cmp_eq_u32 s[62:63], -1, v27\n"
    "v_cmp_eq_u32 s[64:65], -1, v31\n"
    "v_cmp_eq_u32 s[66:67], -1, v35\n"
    "v_cmp_eq_u32 s[68:69], -1, v39\n"
    "v_cmp_eq_u32 s[70:71], -1, v43\n"
    "s_mov_b64 exec, s[62:63]\n"
    "buffer_load_dwordx4 v[24:27], v19, s[40:43], 0 offen sc1\n"
    "s_mov_b64 exec, s[64:65]\n"
    "buffer_load_dwordx4 v[28:31], v19, s[40:43], s59 offen sc1\n"
    "s_mov_b64 exec, s[66:67]\n"
    "buffer_load_dwordx4 v[32:35], v19, s[40:43], s60 offen sc1\n"
    "s_mov_b64 exec, s[68:69]\n"
    "buffer_load_dwordx4 v[36:39], v19, s[40:43], s61 offen sc1\n"
    "s_mov_b64 exec, s[70:71]\n"
    "buffer_load_dwordx4 v[40:43], v20, s[40:43], 0 offen sc1\n"
    "s_mov_b64 exec, -1\n"
    ".Lk_c1:\n"
    "s_waitcnt vmcnt(10)\n"
    "v_max3_u32 v44, v27, v31, v35\n"
    "v_max3_u32 v44, v44, v39, v43\n"
    "v_cmp_eq_u32 vcc, -1, v44\n"
    "s_cbranch_vccnz .Lk_c0\n"
    ".Lk_c2:\n"
    "v_pk_add_f32 v[48:49], v[24:25], v[28:29]\n"
    "v_pk_add_f32 v[50:51], v[26:27], v[30:31]\n"
    "v_pk_add_f32 v[48:49], v[48:49], v[32:33]\n"
    "v_pk_add_f32 v[50:51], v[50:51], v[34:35]\n"
    "v_pk_add_f32 v[48:49], v[48:49], v[36:37]\n"
    "v_pk_add_f32 v[50:51], v[50:51], v[38:39]\n"
    "v_pk_add_f32 v[48:49], v[48:49], v[40:41]\n"
    "v_pk_add_f32 v[50:51], v[50:51], v[42:43]\n"
    "s_nop 1\n"
    "v_permlane32_swap_b32 v48, v50\n"
    "v_permlane32_swap_b32 v49, v51\n"
    "v_pk_add_f32 v[48:49], v[48:49], v[50:51]\n"
    "s_nop 1\n"
    "v_permlane16_swap_b32 v48, v49\n"
    "v_add_f32 v48, v48, v49\n"
    "s_nop 1\n"
    "v_add_f32_dpp v48, v48, v48 row_ror:4 row_mask:0xf bank_mask:0xf\n"
    "s_nop 1\n"
    "v_add_f32_dpp v48, v48, v48 row_ror:8 row_mask:0xf bank_mask:0xf\n"
    "v_cmp_u_f32 vcc, v48, v48\n"
    "s_cbranch_vccnz .Lk_cnan\n"
    ".Lk_cok:\n"
    "v_cvt_pk_bf16_f32 v48, v48, v48\n"
    "v_lshlrev_b32 v48, 16, v48\n"
    "v_mul_f32 v48, v16, v48\n"
    "v_cvt_pk_bf16_f32 v48, v48, v48\n"
    "v_lshlrev_b32 v48, 16, v48\n"
    "v_add_f32 v48, v13, v48\n"
    "v_cvt_pk_bf16_f32 v48, v48, v48\n"
    "s_cbranch_execz .Lk_end\n"
    "buffer_store_short v48, v21, s[48:51], 0 offen\n"
    "s_branch .Lk_end\n"
    ".Lk_cnan:\n"
    "s_waitcnt vmcnt(0)\n"
    "v_mov_b32 v44, 0\n"
    "v_max3_u32 v44, v44, v24, v25\n"
    "v_max3_u32 v44, v44, v26, v27\n"
    "v_max3_u32 v44, v44, v28, v29\n"
    "v_max3_u32 v44, v44, v30, v31\n"
    "v_max3_u32 v44, v44, v32, v33\n"
    "v_max3_u32 v44, v44, v34, v35\n"
    "v_max3_u32 v44, v44, v36, v37\n"
    "v_max3_u32 v44, v44, v38, v39\n"
    "v_max3_u32 v44, v44, v40, v41\n"
    "v_max3_u32 v44, v44, v42, v43\n"
    "v_cmp_eq_u32 vcc, -1, v44\n"
    "s_cbranch_vccnz .Lknp109\n"
    "s_cmp_lg_u32 s72, 0\n"
    "s_mov_b32 s72, 1\n"
    "s_cbranch_scc1 .Lk_cok\n"
    "s_branch .Lk_c2\n"
    ".Lknp109:\n"
    "s_cmp_eq_u32 s38, 0\n"
    "s_cbranch_scc1 .Lk_cok\n"
    "s_sub_u32 s38, s38, 1\n"
    "s_branch .Lk_call\n"
    ".Lk_qpoll:\n"
    ".Lk_q0:\n"
    "s_lshl_b32 s3, s27, 10\n"
    "s_add_u32 s3, s3, 14848\n"
    "v_lshlrev_b32 v98, 3, v1\n"
    "v_add_u32 v99, s3, v91\n"
    "v_and_b32 v96, 48, v91\n"
    "s_mov_b32 s66, 0\n"
    "s_mov_b32 s67, -60\n"
    "s_setprio 3\n"
    "s_branch .Lkut111\n"
    ".Lkut110:\n"
    "s_sleep 1\n"
    ".Lkut111:\n"
    "s_memrealtime s[76:77]\n"
    "s_waitcnt lgkmcnt(0)\n"
    "s_sub_u32 s76, s76, s74\n"
    "s_cmp_lt_i32 s76, 620\n"
    "s_cbranch_scc1 .Lkut110\n"
    "buffer_load_dwordx4 v[104:107], v96, s[40:43], 0 offen offset:512 sc1\n"
    ".Lk_q2:\n"
    "buffer_load_dwordx4 v[100:103], v99, s[40:43], s66 offen sc1\n"
    "s_waitcnt vmcnt(0)\n"
    "v_add_u32 v97, 1, v112\n"
    "v_cmp_ne_u32 vcc, v101, v97\n"
    "v_cmp_ne_u32 s[64:65], v103, v97\n"
    "s_or_b64 vcc, vcc, s[64:65]\n"
    "s_cbranch_vccnz .Lk_q1\n"
    ".Lk_q3:\n"
    "v_subrev_u32 v104, s74, v104\n"
    "v_subrev_u32 v106, s74, v106\n"
    "v_add_u32 v106, v106, v104\n"
    "v_cmp_ne_u32 vcc, v107, v97\n"
    "v_cmp_ne_u32 s[64:65], v105, v97\n"
    "s_or_b64 vcc, vcc, s[64:65]\n"
    "v_add_u32_dpp v106, v106, v106 quad_perm:[1,0,3,2] row_mask:0xf bank_mask:0xf\n"
    "s_nop 1\n"
    "v_add_u32_dpp v106, v106, v106 quad_perm:[2,3,0,1] row_mask:0xf bank_mask:0xf\n"
    "v_ashrrev_i32 v106, 3, v106\n"
    "v_add_u32 v106, 8, v106\n"
    "v_med3_i32 v106, v106, s67, 60\n"
    "s_cbranch_vccz .Lk_tok\n"
    "v_mov_b32 v106, 0\n"
    ".Lk_tok:\n"
    "v_add_u32 v106, s74, v106\n"
    "ds_write_b32 v125, v106 offset:816\n"
    "ds_write2_b32 v98, v100, v102 offset0:208 offset1:209\n"
    "ds_write_b32 v125, v97 offset:52\n"
    "ds_write_b32 v125, v124 offset:48\n"
    "s_setprio 1\n"
    "s_branch .Lk_qwait\n"
    ".Lk_q1:\n"
    "s_cmp_eq_u32 s66, 0\n"
    "s_cselect_b32 s66, 530432, s66\n"
    "s_add_u32 s66, s66, 0x8000\n"
    "s_min_u32 s66, s66, 628736\n"
    "s_sleep 16\n"
    "s_cmp_eq_u32 s38, 0\n"
    "s_cbranch_scc1 .Lk_q3\n"
    "s_sub_u32 s38, s38, 1\n"
    "s_branch .Lk_q2\n"
    ".Lk_hw2:\n"
    "s_cbranch_execz .Lk_end\n"
    "s_bfe_u32 s54, s29, 0x70017\n"
    "s_lshr_b32 s55, s30, 24\n"
    "s_bitcmp1_b32 s36, 0\n"
    "s_cselect_b32 s54, s55, s54\n"
    "s_add_u32 s54, s54, 1\n"
    "s_mul_i32 s54, s54, 73728\n"
    "s_lshr_b32 s3, s36, 1\n"
    "s_mul_i32 s3, s3, 9216\n"
    "s_add_u32 s54, s54, s3\n"
    "s_sub_u32 s54, s54, 18432\n"
    "s_mov_b64 s[44:45], s[22:23]\n"
    "s_mov_b32 s46, 5308416\n"
    "v_lshlrev_b32 v4, 7, v1\n"
    "s_mov_b32 m0, 1344\n"
    "s_branch .Lkfw113\n"
    ".Lkfw112:\n"
    "s_sleep 1\n"
    "s_cmp_eq_u32 s38, 0\n"
    "s_cbranch_scc1 .Lkfw114\n"
    "s_sub_u32 s38, s38, 1\n"
    ".Lkfw113:\n"
    "ds_read_b32 v6, v125 offset:60\n"
    "s_waitcnt lgkmcnt(0)\n"
    "v_cmp_eq_u32 vcc, 0, v6\n"
    "s_cbranch_vccnz .Lkfw112\n"
    ".Lkfw114:\n"
    "s_sleep 24\n"
    "s_mov_b32 exec_hi, 15\n"
    "buffer_load_dword v4, s[44:47], s54 offen lds\n"
    "s_addk_i32 s54, 4608\n"
    "buffer_load_dword v4, s[44:47], s54 offen lds\n"
    "s_addk_i32 s54, 4608\n"
    "s_mov_b64 exec, -1\n"
    "s_branch .Lk_end\n"
    ".Lk_extra:\n"
    "s_sub_u32 s83, s83, 20\n"
    "s_cmp_lg_u32 s37, 1\n"
    "s_cbranch_scc1 .Lk_noextra\n"
    "s_cmp_eq_u32 s26, 7\n"
    "s_cbranch_scc1 .Lk_noextra\n"
    "v_lshrrev_b32 v6, 3, v1\n"
    "v_and_b32 v4, 6, v6\n"
    "v_and_b32 v5, 7, v1\n"
    "v_xor_b32 v4, v4, v5\n"
    "v_lshlrev_b32 v4, 4, v4\n"
    "s_mov_b64 s[44:45], s[20:21]\n"
    "s_mov_b32 s46, 4718592\n"
    "s_lshl_b32 s3, s27, 2\n"
    "s_add_u32 s3, s3, s36\n"
    "s_mul_i32 s58, s3, 73728\n"
    "s_lshl_b32 s3, s33, 7\n"
    "s_add_u32 s58, s58, s3\n"
    "s_add_u32 s59, s58, 0x2000\n"
    "s_add_u32 s60, s58, 0x4000\n"
    "s_add_u32 s61, s58, 0x6000\n"
    "s_lshl_b32 s3, s36, 13\n"
    "s_add_u32 m0, s3, 98304\n"
    "v_mul_u32_u24 v6, 1152, v6\n"
    "v_add_u32 v4, v4, v6\n"
    "v_add_u32 v6, 36864, v4\n"
    "s_mul_i32 s82, s36, 5\n"
    "s_add_u32 s82, s82, s83\n"
    "s_mul_i32 s82, s82, 930\n"
    "s_ashr_i32 s82, s82, 8\n"
    "s_branch .Lkut116\n"
    ".Lkut115:\n"
    "s_sleep 1\n"
    ".Lkut116:\n"
    "s_memrealtime s[76:77]\n"
    "s_waitcnt lgkmcnt(0)\n"
    "s_sub_u32 s76, s76, s74\n"
    "s_cmp_lt_i32 s76, s82\n"
    "s_cbranch_scc1 .Lkut115\n"
    "buffer_load_dwordx4 v4, s[44:47], s58 offen offset:0 nt lds\n"
    "buffer_load_dwordx4 v4, s[44:47], s59 offen offset:1024 nt lds\n"
    "buffer_load_dwordx4 v4, s[44:47], s60 offen offset:2048 nt lds\n"
    "buffer_load_dwordx4 v4, s[44:47], s61 offen offset:3072 nt lds\n"
    "s_add_u32 m0, m0, 0x1000\n"
    "s_nop 0\n"
    "buffer_load_dwordx4 v6, s[44:47], s58 offen offset:0 nt lds\n"
    "s_branch .Lk_noextra\n"
    ".Lk_head:\n"
    "buffer_load_dwordx4 v[116:119], v125, s[40:43], 0 offen offset:0 sc1\n"
    "s_and_b32 s35, s29, 127\n"
    "v_mov_b32 v124, -1\n"
    "v_mov_b32 v125, -1\n"
    "v_mov_b32 v126, -1\n"
    "v_mov_b32 v127, -1\n"
    "s_bitcmp1_b32 s28, 27\n"
    "s_cbranch_scc0 .Lk_clrb\n"
    "s_lshl_b32 s3, s35, 12\n"
    "s_lshl_b32 s54, s26, 10\n"
    "s_add_u32 s3, s3, s54\n"
    "s_add_u32 s54, s3, 116736\n"
    "buffer_store_dwordx4 v[124:127], v91, s[40:43], s54 offen sc1\n"
    "s_lshr_b32 s3, s3, 2\n"
    "s_add_u32 s3, s3, 51200\n"
    "s_mov_b64 exec, 0xffff\n"
    "buffer_store_dwordx4 v[124:127], v91, s[40:43], s3 offen sc1\n"
    "s_mov_b64 exec, -1\n"
    "s_branch .Lk_clrd\n"
    ".Lk_clrb:\n"
    "s_cmp_gt_u32 s35, 17\n"
    "s_cbranch_scc1 .Lk_clrd\n"
    "s_lshl_b32 s3, s35, 2\n"
    "s_add_u32 s3, s3, s26\n"
    "s_lshl_b32 s3, s3, 8\n"
    "s_add_u32 s3, s3, 32768\n"
    "s_mov_b64 exec, 0xffff\n"
    "buffer_store_dwordx4 v[124:127], v91, s[40:43], s3 offen sc1\n"
    "s_mov_b64 exec, -1\n"
    ".Lk_clrd:\n"
    "v_bfe_u32 v80, v1, 3, 2\n"
    "v_xor_b32 v80, s36, v80\n"
    "v_lshlrev_b32 v80, 5, v80\n"
    "v_lshrrev_b32 v6, 2, v1\n"
    "v_lshl_add_u32 v6, v6, 7, v80\n"
    "v_and_b32 v80, 3, v1\n"
    "v_lshl_add_u32 v6, v80, 3, v6\n"
    "v_add_u32 v6, 24576, v6\n"
    "v_add_u32 v7, 0x10000, v6\n"
    "v_and_b32 v5, 3, v1\n"
    "v_lshlrev_b32 v5, 2, v5\n"
    "v_and_b32 v4, 48, v1\n"
    "s_bitcmp1_b32 s28, 27\n"
    "s_cselect_b32 s3, 54, 0\n"
    "s_add_u32 s3, s3, s35\n"
    "s_lshl_b32 s3, s3, 8\n"
    "s_lshl_b32 s54, s26, 6\n"
    "s_add_u32 s3, s3, s54\n"
    "s_add_u32 s3, s3, 1024\n"
    "v_add_u32 v112, s3, v4\n"
    "v_and_b32 v114, 15, v1\n"
    "v_sub_u32 v114, 3, v114\n"
    "v_lshlrev_b32 v113, 15, v114\n"
    "v_add_u32 v113, 530432, v113\n"
    "v_cmp_eq_u32 vcc, 0, v114\n"
    "s_nop 1\n"
    "v_cndmask_b32 v113, v113, 0, vcc\n"
    "v_add_u32 v112, v112, v113\n"
    "s_bitcmp1_b32 s28, 30\n"
    "s_cselect_b32 s3, 4, 1\n"
    "v_mov_b32 v113, 0x7ffffff0\n"
    "v_cmp_gt_u32 vcc, s3, v114\n"
    "s_nop 1\n"
    "v_cndmask_b32 v112, v113, v112, vcc\n"
    "v_min_u32 v113, 8, v5\n"
    "s_waitcnt vmcnt(0)\n"
    "ds_write_b32 v2, v124 offset:0\n"
    "v_add_u32 v121, 1, v116\n"
    "v_add_u32 v123, 1, v116\n"
    "s_cbranch_execz .Lk_nots\n"
    "s_lshl_b32 s3, s26, 8\n"
    "s_add_u32 s3, s3, s2\n"
    "s_cmp_gt_u32 s3, 7\n"
    "s_cbranch_scc1 .Lk_nots\n"
    "s_waitcnt lgkmcnt(0)\n"
    "s_lshl_b32 s3, s3, 3\n"
    "v_mov_b32 v120, s74\n"
    "s_mov_b64 exec, 1\n"
    "buffer_store_dwordx2 v[120:121], off, s[40:43], s3 offset:512 sc1\n"
    "s_mov_b64 exec, -1\n"
    ".Lk_nots:\n"
    "s_branch .Lkfw118\n"
    ".Lkfw117:\n"
    "s_cmp_eq_u32 s38, 0\n"
    "s_cbranch_scc1 .Lkfw119\n"
    "s_sub_u32 s38, s38, 1\n"
    ".Lkfw118:\n"
    "ds_read_b32 v80, v5 offset:0\n"
    "s_waitcnt lgkmcnt(0)\n"
    "v_cmp_eq_u32 vcc, 0, v80\n"
    "s_cbranch_vccnz .Lkfw117\n"
    ".Lkfw119:\n"
    "ds_read_b64_tr_b16 v[8:9], v6 offset:0\n"
    "ds_read_b64_tr_b16 v[10:11], v6 offset:2048\n"
    "ds_read_b64_tr_b16 v[12:13], v6 offset:4096\n"
    "ds_read_b64_tr_b16 v[14:15], v6 offset:6144\n"
    "ds_read_b64_tr_b16 v[16:17], v6 offset:8192\n"
    "ds_read_b64_tr_b16 v[18:19], v6 offset:10240\n"
    "ds_read_b64_tr_b16 v[20:21], v6 offset:12288\n"
    "ds_read_b64_tr_b16 v[22:23], v6 offset:14336\n"
    "ds_read_b64_tr_b16 v[24:25], v6 offset:16384\n"
    "ds_read_b64_tr_b16 v[26:27], v6 offset:18432\n"
    "ds_read_b64_tr_b16 v[28:29], v6 offset:20480\n"
    "ds_read_b64_tr_b16 v[30:31], v6 offset:22528\n"
    "ds_read_b64_tr_b16 v[32:33], v6 offset:24576\n"
    "ds_read_b64_tr_b16 v[34:35], v6 offset:26624\n"
    "ds_read_b64_tr_b16 v[36:37], v6 offset:28672\n"
    "ds_read_b64_tr_b16 v[38:39], v6 offset:30720\n"
    "ds_read_b64_tr_b16 v[40:41], v6 offset:32768\n"
    "ds_read_b64_tr_b16 v[42:43], v6 offset:34816\n"
    "ds_read_b64_tr_b16 v[44:45], v6 offset:36864\n"
    "ds_read_b64_tr_b16 v[46:47], v6 offset:38912\n"
    "ds_read_b64_tr_b16 v[48:49], v6 offset:40960\n"
    "ds_read_b64_tr_b16 v[50:51], v6 offset:43008\n"
    "ds_read_b64_tr_b16 v[52:53], v6 offset:45056\n"
    "ds_read_b64_tr_b16 v[54:55], v6 offset:47104\n"
    "ds_read_b64_tr_b16 v[56:57], v6 offset:49152\n"
    "ds_read_b64_tr_b16 v[58:59], v6 offset:51200\n"
    "ds_read_b64_tr_b16 v[60:61], v6 offset:53248\n"
    "ds_read_b64_tr_b16 v[62:63], v6 offset:55296\n"
    "ds_read_b64_tr_b16 v[64:65], v6 offset:57344\n"
    "ds_read_b64_tr_b16 v[66:67], v6 offset:59392\n"
    "ds_read_b64_tr_b16 v[68:69], v6 offset:61440\n"
    "ds_read_b64_tr_b16 v[70:71], v6 offset:63488\n"
    "ds_read_b64_tr_b16 v[72:73], v7 offset:0\n"
    "ds_read_b64_tr_b16 v[74:75], v7 offset:2048\n"
    "ds_read_b64_tr_b16 v[76:77], v7 offset:4096\n"
    "ds_read_b64_tr_b16 v[78:79], v7 offset:6144\n"
    "s_branch .Lkfw121\n"
    ".Lkfw120:\n"
    "s_cmp_eq_u32 s38, 0\n"
    "s_cbranch_scc1 .Lkfw122\n"
    "s_sub_u32 s38, s38, 1\n"
    ".Lkfw121:\n"
    "ds_read_b32 v80, v113 offset:16\n"
    "s_waitcnt lgkmcnt(0)\n"
    "v_cmp_eq_u32 vcc, 0, v80\n"
    "s_cbranch_vccnz .Lkfw120\n"
    ".Lkfw122:\n"
    "ds_read_b128 v[80:83], v4 offset:2880\n"
    "ds_read_b128 v[84:87], v4 offset:2944\n"
    "ds_read_b128 v[88:91], v4 offset:3008\n"
    "ds_read_b128 v[92:95], v4 offset:3072\n"
    "ds_read_b128 v[96:99], v4 offset:3136\n"
    "ds_read_b128 v[100:103], v4 offset:3200\n"
    "ds_read_b128 v[104:107], v4 offset:3264\n"
    "ds_read_b128 v[108:111], v4 offset:3328\n"
    "s_waitcnt lgkmcnt(7)\n"
    "v_mfma_f32_16x16x32_bf16 v[116:119], v[8:11], v[80:83], 0\n"
    "ds_read_b128 v[80:83], v4 offset:3392\n"
    "s_waitcnt lgkmcnt(6)\n"
    "v_mfma_f32_16x16x32_bf16 v[116:119], v[12:15], v[84:87], v[116:119]\n"
    "ds_read_b128 v[84:87], v4 offset:3456\n"
    "v_mfma_f32_16x16x32_bf16 v[116:119], v[16:19], v[88:91], v[116:119]\n"
    "ds_read_b128 v[88:91], v4 offset:3520\n"
    "s_waitcnt lgkmcnt(6)\n"
    "v_mfma_f32_16x16x32_bf16 v[116:119], v[20:23], v[92:95], v[116:119]\n"
    "ds_read_b128 v[92:95], v4 offset:3584\n"
    "v_mfma_f32_16x16x32_bf16 v[116:119], v[24:27], v[96:99], v[116:119]\n"
    "ds_read_b128 v[96:99], v4 offset:3648\n"
    "s_waitcnt lgkmcnt(6)\n"
    "v_mfma_f32_16x16x32_bf16 v[116:119], v[28:31], v[100:103], v[116:119]\n"
    "ds_read_b128 v[100:103], v4 offset:3712\n"
    "v_mfma_f32_16x16x32_bf16 v[116:119], v[32:35], v[104:107], v[116:119]\n"
    "ds_read_b128 v[104:107], v4 offset:3776\n"
    "s_waitcnt lgkmcnt(6)\n"
    "v_mfma_f32_16x16x32_bf16 v[116:119], v[36:39], v[108:111], v[116:119]\n"
    "ds_read_b128 v[108:111], v4 offset:3840\n"
    "v_mfma_f32_16x16x32_bf16 v[116:119], v[40:43], v[80:83], v[116:119]\n"
    "ds_read_b128 v[80:83], v4 offset:3904\n"
    "s_waitcnt lgkmcnt(6)\n"
    "v_mfma_f32_16x16x32_bf16 v[116:119], v[44:47], v[84:87], v[116:119]\n"
    "ds_read_b128 v[84:87], v4 offset:3968\n"
    "v_mfma_f32_16x16x32_bf16 v[116:119], v[48:51], v[88:91], v[116:119]\n"
    "s_waitcnt lgkmcnt(5)\n"
    "v_mfma_f32_16x16x32_bf16 v[116:119], v[52:55], v[92:95], v[116:119]\n"
    "v_mfma_f32_16x16x32_bf16 v[116:119], v[56:59], v[96:99], v[116:119]\n"
    "s_waitcnt lgkmcnt(3)\n"
    "v_mfma_f32_16x16x32_bf16 v[116:119], v[60:63], v[100:103], v[116:119]\n"
    "v_mfma_f32_16x16x32_bf16 v[116:119], v[64:67], v[104:107], v[116:119]\n"
    "s_waitcnt lgkmcnt(1)\n"
    "v_mfma_f32_16x16x32_bf16 v[116:119], v[68:71], v[108:111], v[116:119]\n"
    "v_mfma_f32_16x16x32_bf16 v[116:119], v[72:75], v[80:83], v[116:119]\n"
    "s_waitcnt lgkmcnt(0)\n"
    "v_mfma_f32_16x16x32_bf16 v[116:119], v[76:79], v[84:87], v[116:119]\n"
    "s_cbranch_execz .Lk_m12\n"
    "s_nop 6\n"
    "v_cvt_pk_bf16_f32 v120, v116, v117\n"
    "v_cvt_pk_bf16_f32 v122, v118, v119\n"
    "buffer_store_dwordx4 v[120:123], v112, s[40:43], 0 offen sc1\n"
    "s_branch .Lk_kv\n"
    ".Lk_act:\n"
    "s_bitcmp1_b32 s28, 27\n"
    "v_mov_b32 v124, -1\n"
    "v_lshlrev_b32 v9, 4, v8\n"
    "v_add_u32 v9, 1024, v9\n"
    "v_lshrrev_b32 v6, 3, v8\n"
    "v_lshlrev_b32 v6, 6, v6\n"
    "v_and_b32 v7, 3, v8\n"
    "v_lshl_add_u32 v6, v7, 4, v6\n"
    "v_and_b32 v7, 4, v8\n"
    "v_lshl_add_u32 v6, v7, 1, v6\n"
    "s_waitcnt vmcnt(0)\n"
    "v_add_u32 v121, 1, v121\n"
    "v_lshlrev_b32 v12, 16, v10\n"
    "v_and_b32 v13, 0xffff0000, v10\n"
    "v_lshlrev_b32 v14, 16, v11\n"
    "v_and_b32 v15, 0xffff0000, v11\n"
    "s_cbranch_scc1 .Lk_ln\n"
    "v_mul_f32 v16, 0xbfb8aa3b, v12\n"
    "v_mul_f32 v17, 0xbfb8aa3b, v13\n"
    "v_mul_f32 v18, 0xbfb8aa3b, v14\n"
    "v_mul_f32 v19, 0xbfb8aa3b, v15\n"
    "v_exp_f32 v16, v16\n"
    "v_exp_f32 v17, v17\n"
    "v_exp_f32 v18, v18\n"
    "v_exp_f32 v19, v19\n"
    "v_add_f32 v16, 1.0, v16\n"
    "v_add_f32 v17, 1.0, v17\n"
    "v_add_f32 v18, 1.0, v18\n"
    "v_add_f32 v19, 1.0, v19\n"
    "v_rcp_f32 v16, v16\n"
    "v_rcp_f32 v17, v17\n"
    "v_rcp_f32 v18, v18\n"
    "v_rcp_f32 v19, v19\n"
    "v_mul_f32 v12, v12, v16\n"
    "v_mul_f32 v13, v13, v17\n"
    "v_mul_f32 v14, v14, v18\n"
    "v_mul_f32 v15, v15, v19\n"
    "s_branch .Lk_actw\n"
    ".Lk_ln:\n"
    "v_lshlrev_b32 v24, 16, v16\n"
    "v_and_b32 v25, 0xffff0000, v16\n"
    "v_lshlrev_b32 v26, 16, v17\n"
    "v_and_b32 v27, 0xffff0000, v17\n"
    "v_lshlrev_b32 v28, 16, v18\n"
    "v_and_b32 v29, 0xffff0000, v18\n"
    "v_lshlrev_b32 v30, 16, v19\n"
    "v_and_b32 v31, 0xffff0000, v19\n"
    "v_lshlrev_b32 v32, 16, v20\n"
    "v_and_b32 v33, 0xffff0000, v20\n"
    "v_lshlrev_b32 v34, 16, v21\n"
    "v_and_b32 v35, 0xffff0000, v21\n"
    "v_lshlrev_b32 v36, 16, v22\n"
    "v_and_b32 v37, 0xffff0000, v22\n"
    "v_lshlrev_b32 v38, 16, v23\n"
    "v_and_b32 v39, 0xffff0000, v23\n"
    "v_add_f32 v40, v24, v25\n"
    "v_add_f32 v40, v40, v26\n"
    "v_add_f32 v40, v40, v27\n"
    "v_add_f32 v40, v40, v28\n"
    "v_add_f32 v40, v40, v29\n"
    "v_add_f32 v40, v40, v30\n"
    "v_add_f32 v40, v40, v31\n"
    "v_add_f32 v40, v40, v32\n"
    "v_add_f32 v40, v40, v33\n"
    "v_add_f32 v40, v40, v34\n"
    "v_add_f32 v40, v40, v35\n"
    "v_add_f32 v40, v40, v36\n"
    "v_add_f32 v40, v40, v37\n"
    "v_add_f32 v40, v40, v38\n"
    "v_add_f32 v40, v40, v39\n"
    "s_nop 1\n"
    "v_add_f32_dpp v40, v40, v40 row_ror:1 row_mask:0xf bank_mask:0xf\n"
    "s_nop 1\n"
    "v_add_f32_dpp v40, v40, v40 row_ror:2 row_mask:0xf bank_mask:0xf\n"
    "s_nop 1\n"
    "v_add_f32_dpp v40, v40, v40 row_ror:4 row_mask:0xf bank_mask:0xf\n"
    "s_nop 1\n"
    "v_add_f32_dpp v40, v40, v40 row_ror:8 row_mask:0xf bank_mask:0xf\n"
    "v_mov_b32 v41, v40\n"
    "s_nop 1\n"
    "v_permlane16_swap_b32 v40, v41\n"
    "v_add_f32 v40, v40, v41\n"
    "v_mov_b32 v41, v40\n"
    "s_nop 1\n"
    "v_permlane32_swap_b32 v40, v41\n"
    "v_add_f32 v40, v40, v41\n"
    "v_mul_f32 v40, 0x3ae38e39, v40\n"
    "v_mov_b32 v42, 0\n"
    "v_mov_b32 v43, 0\n"
    "v_sub_f32 v24, v24, v40\n"
    "v_fmac_f32 v42, v24, v24\n"
    "v_sub_f32 v25, v25, v40\n"
    "v_fmac_f32 v42, v25, v25\n"
    "v_sub_f32 v26, v26, v40\n"
    "v_fmac_f32 v42, v26, v26\n"
    "v_sub_f32 v27, v27, v40\n"
    "v_fmac_f32 v42, v27, v27\n"
    "v_sub_f32 v28, v28, v40\n"
    "v_fmac_f32 v42, v28, v28\n"
    "v_sub_f32 v29, v29, v40\n"
    "v_fmac_f32 v42, v29, v29\n"
    "v_sub_f32 v30, v30, v40\n"
    "v_fmac_f32 v42, v30, v30\n"
    "v_sub_f32 v31, v31, v40\n"
    "v_fmac_f32 v42, v31, v31\n"
    "v_sub_f32 v32, v32, v40\n"
    "v_fmac_f32 v43, v32, v32\n"
    "v_sub_f32 v33, v33, v40\n"
    "v_fmac_f32 v43, v33, v33\n"
    "v_sub_f32 v34, v34, v40\n"
    "v_fmac_f32 v43, v34, v34\n"
    "v_sub_f32 v35, v35, v40\n"
    "v_fmac_f32 v43, v35, v35\n"
    "v_sub_f32 v36, v36, v40\n"
    "v_fmac_f32 v43, v36, v36\n"
    "v_sub_f32 v37, v37, v40\n"
    "v_fmac_f32 v43, v37, v37\n"
    "v_sub_f32 v38, v38, v40\n"
    "v_fmac_f32 v43, v38, v38\n"
    "v_sub_f32 v39, v39, v40\n"
    "v_fmac_f32 v43, v39, v39\n"
    "v_cmp_gt_u32 vcc, 8, v1\n"
    "s_nop 1\n"
    "v_cndmask_b32 v43, 0, v43, vcc\n"
    "v_add_f32 v42, v42, v43\n"
    "s_nop 1\n"
    "v_add_f32_dpp v42, v42, v42 row_ror:1 row_mask:0xf bank_mask:0xf\n"
    "s_nop 1\n"
    "v_add_f32_dpp v42, v42, v42 row_ror:2 row_mask:0xf bank_mask:0xf\n"
    "s_nop 1\n"
    "v_add_f32_dpp v42, v42, v42 row_ror:4 row_mask:0xf bank_mask:0xf\n"
    "s_nop 1\n"
    "v_add_f32_dpp v42, v42, v42 row_ror:8 row_mask:0xf bank_mask:0xf\n"
    "v_mov_b32 v41, v42\n"
    "s_nop 1\n"
    "v_permlane16_swap_b32 v42, v41\n"
    "v_add_f32 v42, v42, v41\n"
    "v_mov_b32 v41, v42\n"
    "s_nop 1\n"
    "v_permlane32_swap_b32 v42, v41\n"
    "v_add_f32 v42, v42, v41\n"
    "v_mul_f32 v42, 0x3ae38e39, v42\n"
    "v_cvt_pk_bf16_f32 v42, v42, v42\n"
    "v_lshlrev_b32 v42, 16, v42\n"
    "v_add_f32 v42, 0x3727c5ac, v42\n"
    "v_cvt_pk_bf16_f32 v42, v42, v42\n"
    "v_lshlrev_b32 v42, 16, v42\n"
    "v_sqrt_f32 v42, v42\n"
    "s_nop 0\n"
    "v_cvt_pk_bf16_f32 v42, v42, v42\n"
    "v_lshlrev_b32 v42, 16, v42\n"
    "v_rcp_f32 v42, v42\n"
    "v_cvt_pk_bf16_f32 v40, v40, v40\n"
    "v_lshlrev_b32 v40, 16, v40\n"
    "v_sub_f32 v12, v12, v40\n"
    "v_cvt_pk_bf16_f32 v12, v12, v12\n"
    "v_lshlrev_b32 v12, 16, v12\n"
    "v_mul_f32 v12, v12, v42\n"
    "v_cvt_pk_bf16_f32 v12, v12, v12\n"
    "v_lshlrev_b32 v12, 16, v12\n"
    "v_sub_f32 v13, v13, v40\n"
    "v_cvt_pk_bf16_f32 v13, v13, v13\n"
    "v_lshlrev_b32 v13, 16, v13\n"
    "v_mul_f32 v13, v13, v42\n"
    "v_cvt_pk_bf16_f32 v13, v13, v13\n"
    "v_lshlrev_b32 v13, 16, v13\n"
    "v_sub_f32 v14, v14, v40\n"
    "v_cvt_pk_bf16_f32 v14, v14, v14\n"
    "v_lshlrev_b32 v14, 16, v14\n"
    "v_mul_f32 v14, v14, v42\n"
    "v_cvt_pk_bf16_f32 v14, v14, v14\n"
    "v_lshlrev_b32 v14, 16, v14\n"
    "v_sub_f32 v15, v15, v40\n"
    "v_cvt_pk_bf16_f32 v15, v15, v15\n"
    "v_lshlrev_b32 v15, 16, v15\n"
    "v_mul_f32 v15, v15, v42\n"
    "v_cvt_pk_bf16_f32 v15, v15, v15\n"
    "v_lshlrev_b32 v15, 16, v15\n"
    "s_branch .Lkut124\n"
    ".Lkut123:\n"
    "s_sleep 1\n"
    ".Lkut124:\n"
    "s_memrealtime s[76:77]\n"
    "s_waitcnt lgkmcnt(0)\n"
    "s_sub_u32 s76, s76, s74\n"
    "s_cmp_lt_i32 s76, 340\n"
    "s_cbranch_scc1 .Lkut123\n"
    "s_setprio 3\n"
    "s_mov_b32 s56, 0\n"
    "s_branch .Lk_m11\n"
    ".Lk_m10:\n"
    "s_cmp_eq_u32 s56, 0\n"
    "s_cselect_b32 s56, 530432, s56\n"
    "s_add_u32 s56, s56, 0x8000\n"
    "s_min_u32 s56, s56, 628736\n"
    "s_sleep 12\n"
    "s_cmp_eq_u32 s38, 0\n"
    "s_cbranch_scc1 .Lk_m12\n"
    "s_sub_u32 s38, s38, 1\n"
    ".Lk_m11:\n"
    "buffer_load_dwordx4 v[16:19], v9, s[40:43], s56 offen sc1\n"
    "buffer_load_dwordx4 v[20:23], v9, s[40:43], s56 offen offset:2304 sc1\n"
    "s_waitcnt vmcnt(0)\n"
    "v_cmp_ne_u32 vcc, v17, v121\n"
    "v_cmp_ne_u32 s[54:55], v19, v121\n"
    "s_or_b64 vcc, vcc, s[54:55]\n"
    "v_cmp_ne_u32 s[54:55], v21, v121\n"
    "s_or_b64 vcc, vcc, s[54:55]\n"
    "v_cmp_ne_u32 s[54:55], v23, v121\n"
    "s_or_b64 vcc, vcc, s[54:55]\n"
    "s_cbranch_vccnz .Lk_m10\n"
    ".Lk_m12:\n"
    "v_lshlrev_b32 v24, 16, v20\n"
    "v_and_b32 v25, 0xffff0000, v20\n"
    "v_add_f32 v24, 1.0, v24\n"
    "v_add_f32 v25, 1.0, v25\n"
    "v_cvt_pk_bf16_f32 v25, v24, v25\n"
    "v_lshlrev_b32 v24, 16, v25\n"
    "v_and_b32 v25, 0xffff0000, v25\n"
    "v_mul_f32 v24, v24, v12\n"
    "v_mul_f32 v25, v25, v13\n"
    "v_cvt_pk_bf16_f32 v25, v24, v25\n"
    "v_lshlrev_b32 v24, 16, v25\n"
    "v_and_b32 v25, 0xffff0000, v25\n"
    "v_lshlrev_b32 v12, 16, v16\n"
    "v_and_b32 v13, 0xffff0000, v16\n"
    "v_add_f32 v12, v12, v24\n"
    "v_add_f32 v13, v13, v25\n"
    "v_lshlrev_b32 v24, 16, v22\n"
    "v_and_b32 v25, 0xffff0000, v22\n"
    "v_add_f32 v24, 1.0, v24\n"
    "v_add_f32 v25, 1.0, v25\n"
    "v_cvt_pk_bf16_f32 v25, v24, v25\n"
    "v_lshlrev_b32 v24, 16, v25\n"
    "v_and_b32 v25, 0xffff0000, v25\n"
    "v_mul_f32 v24, v24, v14\n"
    "v_mul_f32 v25, v25, v15\n"
    "v_cvt_pk_bf16_f32 v25, v24, v25\n"
    "v_lshlrev_b32 v24, 16, v25\n"
    "v_and_b32 v25, 0xffff0000, v25\n"
    "v_lshlrev_b32 v14, 16, v18\n"
    "v_and_b32 v15, 0xffff0000, v18\n"
    "v_add_f32 v14, v14, v24\n"
    "v_add_f32 v15, v15, v25\n"
    ".Lk_actw:\n"
    "v_cvt_pk_bf16_f32 v12, v12, v13\n"
    "v_cvt_pk_bf16_f32 v13, v14, v15\n"
    "ds_write_b64 v6, v[12:13] offset:2880\n"
    "ds_write_b32 v2, v124 offset:0\n"
    "s_cbranch_execz .Lk_mid\n"
    "s_branch .Lk_kv\n"
    ".Lk_dry:\n"
    "s_and_b32 s35, s29, 127\n"
    "s_bfe_u32 s37, s28, 0x20013\n"
    "s_mov_b64 exec, 0\n"
    "s_setprio 0\n"
    "s_cmp_eq_u32 s36, 0\n"
    "s_cbranch_scc1 .Lk_dry0\n"
    "s_cmp_eq_u32 s36, 1\n"
    "s_cbranch_scc1 .Lk_dry1\n"
    "s_cmp_eq_u32 s36, 2\n"
    "s_cbranch_scc1 .Lk_dry2\n"
    "s_bitcmp1_b32 s28, 29\n"
    "s_cbranch_scc1 .Lk_dry4\n"
    "s_branch .Lk_dry3\n"
    ".Lk_dryret:\n"
    "s_mov_b64 exec, -1\n"
    "s_waitcnt lgkmcnt(0)\n"
    "s_branch .Lk_kv\n"
    ".Lk_dry0:\n"
    "s_branch .Lk_gat\n"
    ".Lk_dry1:\n"
    "s_branch .Lk_att\n"
    ".Lk_dry2:\n"
    "s_branch .Lk_g2\n"
    ".Lk_dry3:\n"
    "s_branch .Lk_mid\n"
    ".Lk_dry4:\n"
    "s_branch .Lk_clrd\n"
    ".Lk_end:\n"
    "s_cbranch_execz .Lk_dryret\n"
    ".Lk_fini:\n"
    "s_endpgm\n"
    :
    : "{s[0:1]}"(__builtin_amdgcn_kernarg_segment_ptr()), "{s2}"(__builtin_amdgcn_workgroup_id_x()), "{v0}"(__builtin_amdgcn_workitem_id_x()),
      "i"((__attribute__((address_space(3))) unsigned char*)lds)
    : "memory", "vcc", "scc", "v1", "v2", "v3", "v4", "v5", "v6", "v7", "v8", "v9", "v10", "v11", "v12", "v13", "v14", "v15", "v16", "v17", "v18", "v19", "v20", "v21", "v22", "v23", "v24", "v25", "v26", "v27", "v28", "v29", "v30", "v31", "v32", "v33", "v34", "v35", "v36", "v37", "v38", "v39", "v40", "v41", "v42", "v43", "v44", "v45", "v46", "v47", "v48", "v49", "v50", "v51", "v52", "v53", "v54", "v55", "v56", "v57", "v58", "v59", "v60", "v61", "v62", "v63", "v64", "v65", "v66", "v67", "v68", "v69", "v70", "v71", "v72", "v73", "v74", "v75", "v76", "v77", "v78", "v79", "v80", "v81", "v82", "v83", "v84", "v85", "v86", "v87", "v88", "v89", "v90", "v91", "v92", "v93", "v94", "v95", "v96", "v97", "v98", "v99", "v100", "v101", "v102", "v103", "v104", "v105", "v106", "v107", "v108", "v109", "v110", "v111", "v112", "v113", "v114", "v115", "v116", "v117", "v118", "v119", "v120", "v121", "v122", "v123", "v124", "v125", "v126", "v127", "s3", "s4", "s5", "s6", "s7", "s8", "s9", "s10", "s11", "s12", "s13", "s14", "s15", "s16", "s17", "s18", "s19", "s20", "s21", "s22", "s23", "s24", "s25", "s26", "s27", "s28", "s29", "s30", "s31", "s33", "s34", "s35", "s36", "s37", "s38", "s39", "s40", "s41", "s42", "s43", "s44", "s45", "s46", "s47", "s48", "s49", "s50", "s51", "s52", "s53", "s54", "s55", "s56", "s57", "s58", "s59", "s60", "s61", "s62", "s63", "s64", "s65", "s66", "s67", "s68", "s69", "s70", "s71", "s72", "s73", "s74", "s75", "s76", "s77", "s78", "s79", "s80", "s81", "s82", "s83");
#endif
}

extern "C" void kernel_launch(void* const* d_in, const int* in_sizes, int n_in, void* d_out, int out_size, void* d_ws, size_t ws_size,
                              hipStream_t stream) {
  Args a;
  a.x = d_in[0]; a.K = d_in[1]; a.V = d_in[2]; a.wq = d_in[3]; a.wo = d_in[4];
  a.w1 = d_in[6]; a.w2 = d_in[7]; a.c = d_in[8]; a.wmod = d_in[9];
  a.out = d_out; a.ws = d_ws;
  for (int i = 0; i < 770; ++i) a.tab[i] = kTab[i];
  fused_block<<<256, 1024, 0, stream>>>(a);
}
```
